```python
import torch

def _softmax(x, axis=-1):
    return torch.softmax(x, dim=axis)
def _gelu(x):
    return torch.nn.functional.gelu(x, approximate="tanh")
def _silu(x):
    return torch.nn.functional.silu(x)
def _sigmoid(x):
    return torch.sigmoid(x)
def _layernorm(x, axis=-1, eps=1e-5):
    m = x.mean(dim=axis, keepdim=True)
    v = x.var(dim=axis, keepdim=True, unbiased=False)
    return (x - m) / torch.sqrt(v + eps)
def _scatter_add(tgt, idx, src, axis=0):
    return tgt.clone().index_add_(axis, idx, src)
def _segment_sum(src, seg, n):
    out = torch.zeros((n,) + src.shape[1:], dtype=src.dtype)
    return out.index_add_(0, seg, src)
def _pad(x, pw):
    flat = tuple(v for pair in reversed(pw) for v in pair)
    return torch.nn.functional.pad(x, flat)

kernel_name = 'interleave_layers__sliding_block__swiglu_ffn__prompt'

def setup_inputs():

    _w = (lambda a: (lambda t: t.to(torch.bfloat16) if t.is_floating_point() else t)(torch.as_tensor(a)))
    def _A_setup():
        B, S, D = 1, 2048, 512
        return {
            "x": _w((lambda *s: torch.randn(*s, dtype=torch.bfloat16) * 0.05)(B, S, D)),
            "wqkv": _w((lambda *s: torch.randn(*s, dtype=torch.bfloat16) * 0.05)(D, 3 * D)),
            "wo": _w((lambda *s: torch.randn(*s, dtype=torch.bfloat16) * 0.05)(D, D)),
        }
    _A_in = _A_setup()

    def _B_setup():
        B, S, D, F = 1, 2048, 512, 1792
        return {
            "x": _w((lambda *s: torch.randn(*s, dtype=torch.bfloat16) * 0.05)(B, S, D)),
            "w_gate": _w((lambda *s: torch.randn(*s, dtype=torch.bfloat16) * 0.05)(D, F)),
            "w_up": _w((lambda *s: torch.randn(*s, dtype=torch.bfloat16) * 0.05)(D, F)),
            "w_down": _w((lambda *s: torch.randn(*s, dtype=torch.bfloat16) * 0.05)(F, D)),
        }
    _B_in = _B_setup()

    return {
        "A_x": _A_in["x"],
        "A_wqkv": _A_in["wqkv"],
        "A_wo": _A_in["wo"],
        "B_x": _B_in["x"],
        "B_w_gate": _B_in["w_gate"],
        "B_w_up": _B_in["w_up"],
        "B_w_down": _B_in["w_down"],
    }

def reference(A_x, A_wqkv, A_wo, B_x, B_w_gate, B_w_up, B_w_down):
    def _A_ref(x, wqkv, wo):
        B, S, D = x.shape
        H = 32; Dh = D // H; W = 256
        qkv = (lambda x, *d: x.permute(*d))((x @ wqkv).reshape(B, S, 3, H, Dh), 2, 0, 3, 1, 4)
        q, k, v = qkv[0], qkv[1], qkv[2]
        scores = torch.einsum("bhqd,bhkd->bhqk", q, k) * (Dh ** -0.5)
        idx = torch.arange(S)
        mask = (idx[:, None] - idx[None, :] >= 0) & (idx[:, None] - idx[None, :] < W)
        scores = torch.where(mask[None, None], scores, -1e9)
        p = _softmax(scores, axis=-1)
        o = torch.einsum("bhqk,bhkd->bhqd", p, v)
        return (lambda x, *d: x.permute(*d))(o, 0, 2, 1, 3).reshape(B, S, D) @ wo

    def _B_ref(x, w_gate, w_up, w_down):
        g = _silu(x @ w_gate)
        u = x @ w_up
        return (g * u) @ w_down

    x = A_x
    x = x + _A_ref(_layernorm(x, axis=-1), A_wqkv, A_wo)
    x = x + _B_ref(_layernorm(x, axis=-1), B_w_gate, B_w_up, B_w_down)
    x = x + _A_ref(_layernorm(x, axis=-1), A_wqkv, A_wo)
    x = x + _B_ref(_layernorm(x, axis=-1), B_w_gate, B_w_up, B_w_down)
    return x
```

```cpp
#include <hip/hip_runtime.h>
#include <hip/hip_bf16.h>
#include <stdint.h>

#define GAS __attribute__((address_space(1)))
#define LAS __attribute__((address_space(3)))
typedef unsigned v4u __attribute__((ext_vector_type(4)));
typedef unsigned v2u __attribute__((ext_vector_type(2)));
typedef float f32x4 __attribute__((ext_vector_type(4)));
typedef float f32x2 __attribute__((ext_vector_type(2)));
typedef float f32x16 __attribute__((ext_vector_type(16)));
typedef short bf16x8 __attribute__((ext_vector_type(8)));
typedef __bf16 bf16x2_t __attribute__((ext_vector_type(2)));
#define DI __device__ __forceinline__

constexpr int S_ = 2048, D_ = 512, F_ = 1792;
constexpr int MT = S_ / 32;
constexpr float QSCALE = 0.25f * 1.4426950408889634f;
constexpr float NEGBIG = -1.0e30f;

constexpr size_t WQ_OFF = 0;
constexpr size_t WO_OFF = WQ_OFF + (size_t)16 * 32 * 3 * 1024;
constexpr size_t WG_OFF = WO_OFF + (size_t)8 * 32 * 2 * 1024;
constexpr size_t WD_OFF = WG_OFF + (size_t)16 * 32 * 7 * 1024;
constexpr size_t CS_OFF = WD_OFF + (size_t)8 * 112 * 2 * 1024;
constexpr int CS_N = 1536 + 2 * F_;
constexpr size_t XA_OFF = CS_OFF + (size_t)2 * CS_N * 4 + 0;
constexpr size_t al(size_t x) { return (x + 4095) & ~(size_t)4095; }
constexpr size_t XF_BYTES = (size_t)S_ * D_ * 2;
constexpr size_t ST_BYTES = (size_t)S_ * 16 * 8;
constexpr size_t X_OFF = al(XA_OFF);
constexpr size_t ST_OFF = X_OFF + 4 * XF_BYTES;
constexpr size_t Q_OFF = ST_OFF + 4 * ST_BYTES;
constexpr size_t QKV_BYTES = (size_t)32 * 64 * 1024;
constexpr size_t K_OFF = Q_OFF + 2 * QKV_BYTES;
constexpr size_t V_OFF = K_OFF + 2 * QKV_BYTES;
constexpr size_t O_OFF = V_OFF + 2 * QKV_BYTES;
constexpr size_t H_OFF = O_OFF + 2 * XF_BYTES;
constexpr size_t HF_BYTES = (size_t)S_ * F_ * 2;
constexpr size_t WS_END = H_OFF + 2 * HF_BYTES;

DI float bf2f(unsigned short b) { return __builtin_bit_cast(float, (unsigned)b << 16); }
DI float bflo(unsigned u) { return __builtin_bit_cast(float, u << 16); }
DI float bfhi(unsigned u) { return __builtin_bit_cast(float, u & 0xffff0000u); }
DI unsigned pk2(float lo, float hi) {
  bf16x2_t v;
  v[0] = (__bf16)lo;
  v[1] = (__bf16)hi;
  return __builtin_bit_cast(unsigned, v);
}
DI float rbf(float x) { return (float)(__bf16)x; }
DI f32x16 mfma32(v4u a, v4u b, f32x16 c) {
  return __builtin_amdgcn_mfma_f32_32x32x16_bf16(__builtin_bit_cast(bf16x8, a), __builtin_bit_cast(bf16x8, b), c, 0, 0, 0);
}
DI float swap32(float x) {
  unsigned u = __builtin_bit_cast(unsigned, x);
  auto r = __builtin_amdgcn_permlane32_swap(u, u, false, false);
  return __builtin_bit_cast(float, (threadIdx.x & 32) ? r[0] : r[1]);
}
__host__ __device__ constexpr int rho16(int c) { return (c & 3) | (((c >> 3) & 1) << 2) | (((c >> 2) & 1) << 3); }

DI void transpose8x8(const v4u (&in)[8], v4u (&out)[8]) {
#pragma unroll
  for (int p = 0; p < 4; ++p) {
#pragma unroll
    for (int q = 0; q < 4; ++q) {
      unsigned a = in[2 * q][p], b = in[2 * q + 1][p];
      out[2 * p][q] = __builtin_amdgcn_perm(b, a, 0x05040100u);
      out[2 * p + 1][q] = __builtin_amdgcn_perm(b, a, 0x07060302u);
    }
  }
}

__global__ void __launch_bounds__(256) k_prep(const unsigned short* __restrict__ x, const unsigned short* __restrict__ wqkv,
                                              const unsigned short* __restrict__ wo, const unsigned short* __restrict__ wg,
                                              const unsigned short* __restrict__ wu, const unsigned short* __restrict__ wd,
                                              unsigned char* __restrict__ ws) {
  __shared__ float red[32][65];
  const int b = blockIdx.x, t = threadIdx.x;
  if (b >= 232) {
    const int mt = b - 232, tok = t & 31, part = t >> 5;
    const unsigned short* src = x + (size_t)(mt * 32 + tok) * D_ + part * 64;
    v4u v[8];
#pragma unroll
    for (int i = 0; i < 8; ++i) v[i] = *(const v4u*)(src + i * 8);
    unsigned char* dst = ws + X_OFF + (size_t)mt * 32 * 1024;
#pragma unroll
    for (int i = 0; i < 8; ++i) {
      const int ks = part * 4 + (i >> 1), h = i & 1;
      *(v4u*)(dst + ks * 1024 + (tok + 32 * h) * 16) = v[i];
    }
#pragma unroll
    for (int c = 0; c < 2; ++c) {
      float s = 0.f;
#pragma unroll
      for (int i = 0; i < 4; ++i)
#pragma unroll
        for (int d = 0; d < 4; ++d) s += bflo(v[c * 4 + i][d]) + bfhi(v[c * 4 + i][d]);
      const float m = s * (1.f / 32.f);
      float q = 0.f;
#pragma unroll
      for (int i = 0; i < 4; ++i)
#pragma unroll
        for (int d = 0; d < 4; ++d) {
          float a = bflo(v[c * 4 + i][d]) - m, e = bfhi(v[c * 4 + i][d]) - m;
          q += a * a + e * e;
        }
      *(f32x2*)(ws + ST_OFF + ((size_t)(mt * 32 + tok) * 16 + part * 2 + c) * 8) = f32x2{m, q};
    }
    return;
  }
  const unsigned short* W;
  int N, kind, slab, kc;
  if (b < 48) { W = wqkv; N = 1536; kind = 0; slab = b >> 1; kc = b & 1; }
  else if (b < 104) { W = wg; N = F_; kind = 1; slab = (b - 48) >> 1; kc = b & 1; }
  else if (b < 160) { W = wu; N = F_; kind = 2; slab = (b - 104) >> 1; kc = b & 1; }
  else if (b < 176) { W = wo; N = D_; kind = 3; slab = (b - 160) >> 1; kc = b & 1; }
  else { W = wd; N = D_; kind = 4; slab = (b - 176) / 7; kc = (b - 176) % 7; }
  const int nb = t & 7, kb = t >> 3;
  const int n0 = slab * 64 + nb * 8, k0 = kc * 256 + kb * 8;
  v4u in[8], out[8];
#pragma unroll
  for (int i = 0; i < 8; ++i) in[i] = *(const v4u*)(W + (size_t)(k0 + i) * N + n0);
  transpose8x8(in, out);
  const int ks = k0 >> 4, h = (k0 >> 3) & 1;
  size_t base;
  int rb;
  if (kind == 0) {
    const int part = n0 >> 9, within = n0 & 511, j = within >> 5;
    base = WQ_OFF + ((size_t)(j * 32 + ks) * 3 + part) * 1024;
    rb = within & 16;
  } else if (kind == 1 || kind == 2) {
    const int tt = n0 >> 4, j = tt / 7, i = tt % 7;
    base = WG_OFF + ((size_t)(j * 32 + ks) * 7 + i) * 1024;
    rb = (kind == 2) ? 16 : 0;
  } else if (kind == 3) {
    const int nt = n0 >> 5;
    base = WO_OFF + ((size_t)((nt >> 1) * 32 + ks) * 2 + (nt & 1)) * 1024;
    rb = n0 & 16;
  } else {
    const int nt = n0 >> 5;
    base = WD_OFF + ((size_t)((nt >> 1) * 112 + ks) * 2 + (nt & 1)) * 1024;
    rb = n0 & 16;
  }
  const int beta = (n0 >> 3) & 1;
#pragma unroll
  for (int c = 0; c < 8; ++c) {
    const int rho = rb + (c & 3) + 4 * beta + 8 * (c >> 2);
    *(v4u*)(ws + base + (rho + 32 * h) * 16) = out[c];
  }
  if (kind <= 2) {
#pragma unroll
    for (int c = 0; c < 8; ++c) {
      float s = 0.f;
#pragma unroll
      for (int d = 0; d < 4; ++d) s += bflo(out[c][d]) + bfhi(out[c][d]);
      red[kb][nb * 8 + c] = s;
    }
    __syncthreads();
    if (t < 64) {
      float s = 0.f;
#pragma unroll
      for (int i = 0; i < 32; ++i) s += red[i][t];
      const int cb = (kind == 0) ? 0 : (kind == 1 ? 1536 : 1536 + F_);
      ((float*)(ws + CS_OFF))[kc * CS_N + cb + slab * 64 + t] = s;
    }
  }
}

DI f32x2 row_stats(const unsigned char* st, int tok, int h) {
  const f32x4* p = (const f32x4*)(st + ((size_t)tok * 16 + h * 8) * 8);
  f32x4 v[4];
#pragma unroll
  for (int i = 0; i < 4; ++i) v[i] = p[i];
  float s = 0.f;
#pragma unroll
  for (int i = 0; i < 4; ++i) s += v[i][0] + v[i][2];
  s += swap32(s);
  const float mean = s * (1.f / 16.f);
  float q = 0.f;
#pragma unroll
  for (int i = 0; i < 4; ++i) {
    const float a = v[i][0] - mean, b = v[i][2] - mean;
    q += v[i][1] + v[i][3] + 32.f * (a * a + b * b);
  }
  q += swap32(q);
  const float var = rbf(q * (1.f / 512.f));
  const float sd = rbf(__builtin_sqrtf(rbf(var + 1e-5f)));
  return f32x2{rbf(mean), 1.0f / sd};
}

DI void blk_map16(int b, int& g, int& j) {
  g = 2 * (b & 7) + ((b >> 3) & 1);
  j = b >> 4;
}

template <int NT, int NSWAP>
DI void gemm_msplit(const unsigned char* xf, const unsigned char* wsl, unsigned char* smem, f32x16 (&acc)[NT]) {
  const int tid = threadIdx.x, lane = tid & 63, w = __builtin_amdgcn_readfirstlane(tid >> 6);
  constexpr int CHB = 8 * NT * 1024;
  auto stage = [&](int c, int buf) {
#pragma unroll
    for (int i = 0; i < 2 * NT; ++i) {
      const int piece = i * 4 + w;
      __builtin_amdgcn_global_load_lds((const GAS void*)(wsl + (size_t)c * CHB + piece * 1024 + lane * 16),
                                       (LAS void*)(smem + buf * CHB + piece * 1024), 16, 0, 0);
    }
  };
  stage(0, 0);
  v4u xa[32];
#pragma unroll
  for (int ks = 0; ks < 32; ++ks) xa[ks] = *(const v4u*)(xf + ks * 1024 + lane * 16);
  stage(1, 1);
#pragma unroll
  for (int i = 0; i < NT; ++i)
#pragma unroll
    for (int r = 0; r < 16; ++r) acc[i][r] = 0.f;
#pragma unroll
  for (int c = 0; c < 4; ++c) {
    __syncthreads();
    const unsigned char* bp = smem + (c & 1) * CHB + lane * 16;
#pragma unroll
    for (int k = 0; k < 8; ++k) {
#pragma unroll
      for (int i = 0; i < NT; ++i) {
        const v4u wf = *(const v4u*)(bp + (k * NT + i) * 1024);
        if (i < NSWAP) acc[i] = mfma32(wf, xa[c * 8 + k], acc[i]);
        else acc[i] = mfma32(xa[c * 8 + k], wf, acc[i]);
      }
    }
    if (c + 2 < 4) {
      __syncthreads();
      stage(c + 2, c & 1);
    }
  }
}

__global__ void __launch_bounds__(256, 1) k_qkv(unsigned char* __restrict__ ws, int layer) {
  extern __shared__ __attribute__((aligned(16))) unsigned char smem[];
  int g, j;
  blk_map16(blockIdx.x, g, j);
  const int tid = threadIdx.x, lane = tid & 63, w = __builtin_amdgcn_readfirstlane(tid >> 6), h = lane >> 5, l31 = lane & 31;
  const int mt = 4 * g + w, xb = 2 * layer;
  f32x16 acc[3];
  const f32x2 st = row_stats(ws + ST_OFF + xb * ST_BYTES, mt * 32 + l31, h);
  gemm_msplit<3, 2>(ws + X_OFF + xb * XF_BYTES + (size_t)mt * 32 * 1024, ws + WQ_OFF + (size_t)j * 96 * 1024, smem, acc);
  const float* cs = (const float*)(ws + CS_OFF);
#pragma unroll
  for (int p = 0; p < 2; ++p) {
    const float sc = st[1] * (p == 0 ? QSCALE : 1.f);
#pragma unroll
    for (int hd = 0; hd < 2; ++hd) {
      const int c0 = p * 512 + j * 32 + hd * 16 + 8 * h;
      float o[8];
#pragma unroll
      for (int r = 0; r < 8; ++r) o[r] = (acc[p][hd * 8 + r] - st[0] * (cs[c0 + r] + cs[CS_N + c0 + r])) * sc;
      v4u v = {pk2(o[0], o[1]), pk2(o[2], o[3]), pk2(o[4], o[5]), pk2(o[6], o[7])};
      unsigned char* dst = ws + (p == 0 ? Q_OFF : K_OFF) + layer * QKV_BYTES + ((size_t)(2 * j + hd) * 64 + mt) * 1024 + lane * 16;
      *(v4u*)dst = v;
    }
  }
  {
    __syncthreads();
    f32x2* tb = (f32x2*)(smem + w * 256);
    if (lane < 32) tb[lane] = st;
    __builtin_amdgcn_fence(__ATOMIC_RELEASE, "wavefront");
    __builtin_amdgcn_wave_barrier();
    const int col = 1024 + j * 32 + (l31 & 16) + rho16(l31 & 15);
    const float c = cs[col] + cs[CS_N + col];
#pragma unroll
    for (int s = 0; s < 2; ++s) {
      float o[8];
#pragma unroll
      for (int r = 0; r < 8; ++r) {
        const int tok = 16 * s + 8 * (r >> 2) + 4 * h + (r & 3);
        const f32x2 ts = tb[tok];
        o[r] = (acc[2][8 * s + r] - ts[0] * c) * ts[1];
      }
      v4u v = {pk2(o[0], o[1]), pk2(o[2], o[3]), pk2(o[4], o[5]), pk2(o[6], o[7])};
      unsigned char* dst = ws + V_OFF + layer * QKV_BYTES + ((size_t)(2 * j + (l31 >> 4)) * 64 + mt) * 1024 + s * 512 + ((l31 & 15) + 16 * h) * 16;
      *(v4u*)dst = v;
    }
  }
}

template <bool EDGE>
DI void attn_qtile(const v4u (&kf)[10], const v4u (&vf)[20], int off, v4u qf, int nneg, const f32x16& mlo, const f32x16& mhi, v4u& out) {
  f32x16 zero;
#pragma unroll
  for (int r = 0; r < 16; ++r) zero[r] = 0.f;
  float m = NEGBIG;
#pragma unroll
  for (int u = 0; u < 9; ++u) {
    if (!EDGE || u >= nneg) {
      f32x16 s = mfma32(kf[off + u], qf, u == 0 ? mlo : (u == 8 ? mhi : zero));
#pragma unroll
      for (int r = 0; r < 16; r += 2) m = __builtin_fmaxf(m, __builtin_fmaxf(s[r], s[r + 1]));
    }
  }
  m = __builtin_fmaxf(m, swap32(m));
  f32x16 cm, cl, ch;
#pragma unroll
  for (int r = 0; r < 16; ++r) { cm[r] = -m; cl[r] = mlo[r] - m; ch[r] = mhi[r] - m; }
  f32x16 o = zero;
#pragma unroll
  for (int u = 0; u < 9; ++u) {
    if (!EDGE || u >= nneg) {
      f32x16 s = mfma32(kf[off + u], qf, u == 0 ? cl : (u == 8 ? ch : cm));
      unsigned p[8];
#pragma unroll
      for (int r = 0; r < 8; ++r) p[r] = pk2(__builtin_amdgcn_exp2f(s[2 * r]), __builtin_amdgcn_exp2f(s[2 * r + 1]));
      o = mfma32(vf[2 * (off + u)], v4u{p[0], p[1], p[2], p[3]}, o);
      o = mfma32(vf[2 * (off + u) + 1], v4u{p[4], p[5], p[6], p[7]}, o);
    }
  }
  const float inv = 1.0f / o[8];
  out = v4u{pk2(o[0] * inv, o[1] * inv), pk2(o[2] * inv, o[3] * inv), pk2(o[4] * inv, o[5] * inv), pk2(o[6] * inv, o[7] * inv)};
}

template <bool EDGE>
DI void attn_body(unsigned char* ws, int layer, int g, int j) {
  const int tid = threadIdx.x, lane = tid & 63, w = __builtin_amdgcn_readfirstlane(tid >> 6), h = lane >> 5, l31 = lane & 31;
  const int head = 2 * j + (w & 1), tb0 = 4 * g + 2 * (w >> 1);
  const unsigned char* qb = ws + Q_OFF + layer * QKV_BYTES + (size_t)head * 64 * 1024;
  const unsigned char* kb = ws + K_OFF + layer * QKV_BYTES + (size_t)head * 64 * 1024;
  const unsigned char* vb = ws + V_OFF + layer * QKV_BYTES + (size_t)head * 64 * 1024;
  v4u qf0 = *(const v4u*)(qb + tb0 * 1024 + lane * 16), qf1 = *(const v4u*)(qb + (tb0 + 1) * 1024 + lane * 16);
  v4u kf[10], vf[20];
  const unsigned ones = (l31 == 16 || l31 == 20) ? 0x3f803f80u : 0u;
#pragma unroll
  for (int u = 0; u < 10; ++u) {
    const int tb = EDGE ? max(tb0 - 8 + u, 0) : tb0 - 8 + u;
    kf[u] = *(const v4u*)(kb + tb * 1024 + lane * 16);
  }
#pragma unroll
  for (int u = 0; u < 20; ++u) vf[u] = v4u{ones, ones, ones, ones};
  if (l31 < 16) {
#pragma unroll
    for (int u = 0; u < 10; ++u) {
      const int tb = EDGE ? max(tb0 - 8 + u, 0) : tb0 - 8 + u;
      vf[2 * u] = *(const v4u*)(vb + tb * 1024 + (l31 + 16 * h) * 16);
      vf[2 * u + 1] = *(const v4u*)(vb + tb * 1024 + 512 + (l31 + 16 * h) * 16);
    }
  }
  f32x16 mlo, mhi;
#pragma unroll
  for (int r = 0; r < 16; ++r) {
    const int a = (r & 3) + 8 * (r >> 2) + 4 * h;
    mlo[r] = (a > l31) ? 0.f : NEGBIG;
    mhi[r] = (a <= l31) ? 0.f : NEGBIG;
  }
  v4u o0, o1;
  attn_qtile<EDGE>(kf, vf, 0, qf0, 8 - tb0, mlo, mhi, o0);
  attn_qtile<EDGE>(kf, vf, 1, qf1, 7 - tb0, mlo, mhi, o1);
  unsigned char* ob = ws + O_OFF + layer * XF_BYTES;
  *(v4u*)(ob + ((size_t)tb0 * 32 + head) * 1024 + lane * 16) = o0;
  *(v4u*)(ob + ((size_t)(tb0 + 1) * 32 + head) * 1024 + lane * 16) = o1;
}

__global__ void __launch_bounds__(256, 1) k_attn(unsigned char* __restrict__ ws, int layer) {
  const int b = blockIdx.x, j = 2 * (b & 7) + ((b >> 3) & 1), g = b >> 4;
  if (g < 2) attn_body<true>(ws, layer, g, j);
  else attn_body<false>(ws, layer, g, j);
}

template <int KSW, bool FINAL>
DI void gemm_ksplit(const unsigned char* af, const unsigned char* wsl, const unsigned char* xres, unsigned char* xout,
                    unsigned char* stout, unsigned short* dout, int rb, int sl, unsigned char* smem) {
  const int tid = threadIdx.x, lane = tid & 63, w = __builtin_amdgcn_readfirstlane(tid >> 6), h = lane >> 5, l31 = lane & 31;
  constexpr int KS = 4 * KSW;
  const unsigned char* a0 = af + ((size_t)(2 * rb) * KS + w * KSW) * 1024 + lane * 16;
  const unsigned char* a1 = a0 + (size_t)KS * 1024;
  const unsigned char* wp = wsl + (size_t)(w * KSW) * 2048 + lane * 16;
  const int omt = 2 * rb + (w >> 1), ont = 2 * sl + (w & 1);
  const v4u r0 = *(const v4u*)(xres + ((size_t)omt * 32 + 2 * ont) * 1024 + lane * 16);
  const v4u r1 = *(const v4u*)(xres + ((size_t)omt * 32 + 2 * ont + 1) * 1024 + lane * 16);
  f32x16 acc[4];
#pragma unroll
  for (int i = 0; i < 4; ++i)
#pragma unroll
    for (int r = 0; r < 16; ++r) acc[i][r] = 0.f;
#pragma unroll
  for (int k = 0; k < KSW; ++k) {
    const v4u x0 = *(const v4u*)(a0 + k * 1024), x1 = *(const v4u*)(a1 + k * 1024);
    const v4u w0 = *(const v4u*)(wp + k * 2048), w1 = *(const v4u*)(wp + k * 2048 + 1024);
    acc[0] = mfma32(w0, x0, acc[0]);
    acc[1] = mfma32(w1, x0, acc[1]);
    acc[2] = mfma32(w0, x1, acc[2]);
    acc[3] = mfma32(w1, x1, acc[3]);
  }
#pragma unroll
  for (int i = 0; i < 4; ++i) {
    if (i != w) {
      const int slot = (w - i - 1) & 3;
#pragma unroll
      for (int q = 0; q < 4; ++q)
        *(f32x4*)(smem + (((i * 3 + slot) * 4 + q) * 64 + lane) * 16) = f32x4{acc[i][4 * q], acc[i][4 * q + 1], acc[i][4 * q + 2], acc[i][4 * q + 3]};
    }
  }
  __syncthreads();
  f32x16 o = (w == 0) ? acc[0] : (w == 1) ? acc[1] : (w == 2) ? acc[2] : acc[3];
#pragma unroll
  for (int s = 0; s < 3; ++s)
#pragma unroll
    for (int q = 0; q < 4; ++q) {
      const f32x4 v = *(const f32x4*)(smem + (((w * 3 + s) * 4 + q) * 64 + lane) * 16);
      o[4 * q] += v[0]; o[4 * q + 1] += v[1]; o[4 * q + 2] += v[2]; o[4 * q + 3] += v[3];
    }
  float y[16];
#pragma unroll
  for (int d = 0; d < 4; ++d) {
    y[2 * d] = rbf(bflo(r0[d]) + rbf(o[2 * d]));
    y[2 * d + 1] = rbf(bfhi(r0[d]) + rbf(o[2 * d + 1]));
    y[8 + 2 * d] = rbf(bflo(r1[d]) + rbf(o[8 + 2 * d]));
    y[8 + 2 * d + 1] = rbf(bfhi(r1[d]) + rbf(o[8 + 2 * d + 1]));
  }
  const v4u y0 = {pk2(y[0], y[1]), pk2(y[2], y[3]), pk2(y[4], y[5]), pk2(y[6], y[7])};
  const v4u y1 = {pk2(y[8], y[9]), pk2(y[10], y[11]), pk2(y[12], y[13]), pk2(y[14], y[15])};
  if (FINAL) {
    unsigned short* dp = dout + (size_t)(omt * 32 + l31) * D_ + ont * 32 + 8 * h;
    *(v4u*)dp = y0;
    *(v4u*)(dp + 16) = y1;
  } else {
    *(v4u*)(xout + ((size_t)omt * 32 + 2 * ont) * 1024 + lane * 16) = y0;
    *(v4u*)(xout + ((size_t)omt * 32 + 2 * ont + 1) * 1024 + lane * 16) = y1;
    float s = 0.f;
#pragma unroll
    for (int r = 0; r < 16; ++r) s += y[r];
    s += swap32(s);
    const float m = s * (1.f / 32.f);
    float q = 0.f;
#pragma unroll
    for (int r = 0; r < 16; ++r) q += (y[r] - m) * (y[r] - m);
    q += swap32(q);
    if (h == 0) *(f32x2*)(stout + ((size_t)(omt * 32 + l31) * 16 + ont) * 8) = f32x2{m, q};
  }
}

DI void blk_map32x8(int b, int& rb, int& sl) {
  rb = 4 * (b & 7) + ((b >> 3) & 3);
  sl = b >> 5;
}

__global__ void __launch_bounds__(256, 1) k_wo(unsigned char* __restrict__ ws, int layer) {
  extern __shared__ __attribute__((aligned(16))) unsigned char smem[];
  int rb, sl;
  blk_map32x8(blockIdx.x, rb, sl);
  gemm_ksplit<8, false>(ws + O_OFF + layer * XF_BYTES, ws + WO_OFF + (size_t)sl * 64 * 1024, ws + X_OFF + (2 * layer) * XF_BYTES,
                        ws + X_OFF + (2 * layer + 1) * XF_BYTES, ws + ST_OFF + (2 * layer + 1) * ST_BYTES, nullptr, rb, sl, smem);
}

template <bool FINAL>
__global__ void __launch_bounds__(256, 1) k_down(unsigned char* __restrict__ ws, unsigned short* __restrict__ dout, int layer) {
  extern __shared__ __attribute__((aligned(16))) unsigned char smem[];
  int rb, sl;
  blk_map32x8(blockIdx.x, rb, sl);
  gemm_ksplit<28, FINAL>(ws + H_OFF + layer * HF_BYTES, ws + WD_OFF + (size_t)sl * 224 * 1024, ws + X_OFF + (2 * layer + 1) * XF_BYTES,
                         ws + X_OFF + (2 * layer + 2) * XF_BYTES, ws + ST_OFF + (2 * layer + 2) * ST_BYTES, dout, rb, sl, smem);
}

__global__ void __launch_bounds__(256, 1) k_gu(unsigned char* __restrict__ ws, int layer) {
  extern __shared__ __attribute__((aligned(16))) unsigned char smem[];
  int g, j;
  blk_map16(blockIdx.x, g, j);
  const int tid = threadIdx.x, lane = tid & 63, w = __builtin_amdgcn_readfirstlane(tid >> 6), h = lane >> 5, l31 = lane & 31;
  const int mt = 4 * g + w, xb = 2 * layer + 1;
  f32x16 acc[7];
  const f32x2 st = row_stats(ws + ST_OFF + xb * ST_BYTES, mt * 32 + l31, h);
  gemm_msplit<7, 7>(ws + X_OFF + xb * XF_BYTES + (size_t)mt * 32 * 1024, ws + WG_OFF + (size_t)j * 224 * 1024, smem, acc);
  const float* cs = (const float*)(ws + CS_OFF);
  unsigned char* hb = ws + H_OFF + layer * HF_BYTES + ((size_t)mt * 112 + 7 * j) * 1024 + lane * 16;
#pragma unroll
  for (int i = 0; i < 7; ++i) {
    const int c0 = 1536 + (7 * j + i) * 16 + 8 * h;
    float o[8];
#pragma unroll
    for (int r = 0; r < 8; ++r) {
      const float gt = rbf((acc[i][r] - st[0] * (cs[c0 + r] + cs[CS_N + c0 + r])) * st[1]);
      const float up = rbf((acc[i][8 + r] - st[0] * (cs[F_ + c0 + r] + cs[CS_N + F_ + c0 + r])) * st[1]);
      const float sl = rbf(gt * __builtin_amdgcn_rcpf(1.0f + __builtin_amdgcn_exp2f(-1.4426950408889634f * gt)));
      o[r] = sl * up;
    }
    *(v4u*)(hb + i * 1024) = v4u{pk2(o[0], o[1]), pk2(o[2], o[3]), pk2(o[4], o[5]), pk2(o[6], o[7])};
  }
}

extern "C" void kernel_launch(void* const* d_in, const int* in_sizes, int n_in, void* d_out, int out_size, void* d_ws, size_t ws_size,
                              hipStream_t stream) {
  const unsigned short* x = (const unsigned short*)d_in[0];
  const unsigned short* wqkv = (const unsigned short*)d_in[1];
  const unsigned short* wo = (const unsigned short*)d_in[2];
  const unsigned short* wg = (const unsigned short*)d_in[4];
  const unsigned short* wu = (const unsigned short*)d_in[5];
  const unsigned short* wd = (const unsigned short*)d_in[6];
  unsigned char* ws = (unsigned char*)d_ws;
  unsigned short* out = (unsigned short*)d_out;
  (void)hipFuncSetAttribute((const void*)k_gu, hipFuncAttributeMaxDynamicSharedMemorySize, 112 * 1024);
  k_prep<<<296, 256, 0, stream>>>(x, wqkv, wo, wg, wu, wd, ws);
  for (int layer = 0; layer < 2; ++layer) {
    k_qkv<<<256, 256, 48 * 1024, stream>>>(ws, layer);
    k_attn<<<256, 256, 0, stream>>>(ws, layer);
    k_wo<<<256, 256, 48 * 1024, stream>>>(ws, layer);
    k_gu<<<256, 256, 112 * 1024, stream>>>(ws, layer);
    if (layer == 0) k_down<false><<<256, 256, 48 * 1024, stream>>>(ws, out, layer);
    else k_down<true><<<256, 256, 48 * 1024, stream>>>(ws, out, layer);
  }
}
```

```cpp
#include <hip/hip_runtime.h>
#include <hip/hip_bf16.h>
#include <stdint.h>

#define STAMPS 0

#define GAS __attribute__((address_space(1)))
#define LAS __attribute__((address_space(3)))
typedef unsigned v4u __attribute__((ext_vector_type(4)));
typedef unsigned v2u __attribute__((ext_vector_type(2)));
typedef float f32x4 __attribute__((ext_vector_type(4)));
typedef float f32x2 __attribute__((ext_vector_type(2)));
typedef float f32x16 __attribute__((ext_vector_type(16)));
typedef short bf16x8 __attribute__((ext_vector_type(8)));
typedef __bf16 bf16x2_t __attribute__((ext_vector_type(2)));
typedef GAS unsigned gu32;
typedef __amdgpu_buffer_rsrc_t rsrc_t;
#define DI __device__ __forceinline__
#define RLX_AGENT __ATOMIC_RELAXED, __HIP_MEMORY_SCOPE_AGENT
#define SB() __builtin_amdgcn_sched_barrier(0)

constexpr int S_ = 2048, D_ = 512, F_ = 1792;
constexpr float LOG2E = 1.4426950408889634f;
constexpr float QSCALE = 0.25f * LOG2E;
constexpr float NEGBIG = -1.0e30f;
constexpr int SC1 = 16;

constexpr int CNT_OFF = 0;
constexpr int CNT_BYTES = 10 * 256 * 128;
constexpr int TMO_OFF = CNT_OFF + CNT_BYTES;
constexpr int WQ_OFF = TMO_OFF + 4096;
constexpr int WQ_SL = 33 * 3 * 1024;
constexpr int WO_OFF = WQ_OFF + 16 * WQ_SL;
constexpr int WG_OFF = WO_OFF + 8 * 32 * 2 * 1024;
constexpr int WG_SL = 33 * 7 * 1024;
constexpr int WD_OFF = WG_OFF + 16 * WG_SL;
constexpr int XF_BYTES = S_ * D_ * 2;
constexpr int ST_BYTES = S_ * 16 * 8;
constexpr int X_OFF = WD_OFF + 8 * 112 * 2 * 1024;
constexpr int ST_OFF = X_OFF + 4 * XF_BYTES;
constexpr int QKV_BYTES = 32 * 64 * 1024;
constexpr int Q_OFF = ST_OFF + 4 * ST_BYTES;
constexpr int K_OFF = Q_OFF + 2 * QKV_BYTES;
constexpr int V_OFF = K_OFF + 2 * QKV_BYTES;
constexpr int O_OFF = V_OFF + 2 * QKV_BYTES;
constexpr int HF_BYTES = S_ * F_ * 2;
constexpr int H_OFF = O_OFF + 2 * XF_BYTES;
constexpr int DBG_OFF = H_OFF + 2 * HF_BYTES;
constexpr int WS_END = DBG_OFF + 256 * 64 * 8;

constexpr int L_RED = 112 * 1024;
constexpr int L_MISC = 154 * 1024;
constexpr int LDS_BYTES = 160 * 1024;

DI float bflo(unsigned u) { return __builtin_bit_cast(float, u << 16); }
DI float bfhi(unsigned u) { return __builtin_bit_cast(float, u & 0xffff0000u); }
DI unsigned pk2(float lo, float hi) {
  bf16x2_t v;
  v[0] = (__bf16)lo;
  v[1] = (__bf16)hi;
  return __builtin_bit_cast(unsigned, v);
}
DI float rbf(float x) { return (float)(__bf16)x; }
DI f32x16 mfma32(v4u a, v4u b, f32x16 c) {
  return __builtin_amdgcn_mfma_f32_32x32x16_bf16(__builtin_bit_cast(bf16x8, a), __builtin_bit_cast(bf16x8, b), c, 0, 0, 0);
}
#define MFS "v_mfma_f32_32x32x16_bf16 "
#define MF(acc, CA, a, CB, b) asm volatile(MFS "%0, %1, %2, %0" : "+v"(acc) : CA(a), CB(b))
#define MFN(acc, CA, a, CB, b) asm volatile("s_nop 1\n\t" MFS "%0, %1, %2, %0" : "+v"(acc) : CA(a), CB(b))
#define MF0(acc, CA, a, CB, b) asm volatile(MFS "%0, %1, %2, 0" : "=&v"(acc) : CA(a), CB(b))
#define MF0N(acc, CA, a, CB, b) asm volatile("s_nop 1\n\t" MFS "%0, %1, %2, 0" : "=&v"(acc) : CA(a), CB(b))
#define MFC(d, CA, a, CB, b, c) asm volatile(MFS "%0, %1, %2, %3" : "=&v"(d) : CA(a), CB(b), "v"(c))
#define MFCN(d, CA, a, CB, b, c) asm volatile("s_nop 1\n\t" MFS "%0, %1, %2, %3" : "=&v"(d) : CA(a), CB(b), "v"(c))
#define NOP12() do { asm volatile("s_nop 11"); SB(); } while (0)
DI float swap32(float x, int h) {
  unsigned u = __builtin_bit_cast(unsigned, x);
  auto r = __builtin_amdgcn_permlane32_swap(u, u, false, false);
  return __builtin_bit_cast(float, h ? r[0] : r[1]);
}
DI v4u ldw(rsrc_t rs, int voff, int soff) { return __builtin_amdgcn_raw_buffer_load_b128(rs, voff, soff, 0); }
DI v4u ldx(rsrc_t rs, int voff, int soff) { return __builtin_amdgcn_raw_buffer_load_b128(rs, voff, soff, SC1); }
DI void stx(rsrc_t rs, v4u v, int voff, int soff) { __builtin_amdgcn_raw_buffer_store_b128(v, rs, voff, soff, SC1); }
template <int N> DI void dma(rsrc_t rs, int voff, int soff, int lds) {
  unsigned keep;
  if constexpr (N == 4)
    asm volatile("s_mov_b32 %0, m0\n\ts_mov_b32 m0, %4\n\ts_nop 0\n\tbuffer_load_dwordx4 %1, %2, %3 offen lds\n\t"
                 "buffer_load_dwordx4 %1, %2, %3 offen offset:1024 lds\n\tbuffer_load_dwordx4 %1, %2, %3 offen offset:2048 lds\n\t"
                 "buffer_load_dwordx4 %1, %2, %3 offen offset:3072 lds\n\ts_mov_b32 m0, %0"
                 : "=&s"(keep) : "v"(voff), "s"(rs), "s"(soff), "s"(lds) : "memory");
  else if constexpr (N == 3)
    asm volatile("s_mov_b32 %0, m0\n\ts_mov_b32 m0, %4\n\ts_nop 0\n\tbuffer_load_dwordx4 %1, %2, %3 offen lds\n\t"
                 "buffer_load_dwordx4 %1, %2, %3 offen offset:1024 lds\n\tbuffer_load_dwordx4 %1, %2, %3 offen offset:2048 lds\n\ts_mov_b32 m0, %0"
                 : "=&s"(keep) : "v"(voff), "s"(rs), "s"(soff), "s"(lds) : "memory");
  else if constexpr (N == 2)
    asm volatile("s_mov_b32 %0, m0\n\ts_mov_b32 m0, %4\n\ts_nop 0\n\tbuffer_load_dwordx4 %1, %2, %3 offen lds\n\t"
                 "buffer_load_dwordx4 %1, %2, %3 offen offset:1024 lds\n\ts_mov_b32 m0, %0"
                 : "=&s"(keep) : "v"(voff), "s"(rs), "s"(soff), "s"(lds) : "memory");
  else
    asm volatile("s_mov_b32 %0, m0\n\ts_mov_b32 m0, %4\n\ts_nop 0\n\tbuffer_load_dwordx4 %1, %2, %3 offen lds\n\ts_mov_b32 m0, %0"
                 : "=&s"(keep) : "v"(voff), "s"(rs), "s"(soff), "s"(lds) : "memory");
}
DI int opq(int x);
template <int NP> DI void dma_n(rsrc_t rs, int voff, int soff, int lds) {
  soff = opq(soff);
  lds = opq(lds);
#pragma unroll
  for (int i = 0; i + 4 <= NP; i += 4) dma<4>(rs, voff, soff + i * 1024, lds + i * 1024);
  if constexpr (NP % 4 != 0) dma<NP % 4>(rs, voff, soff + (NP / 4) * 4096, lds + (NP / 4) * 4096);
}
DI int opq(int x) { asm volatile("" : "+s"(x)); return x; }
#define WAIT_VM0() asm volatile("s_waitcnt vmcnt(0)" ::: "memory")
#define BAR_LGKM() asm volatile("s_waitcnt lgkmcnt(0)\n\ts_barrier" ::: "memory")
#define BAR_ALL() asm volatile("s_waitcnt vmcnt(0) lgkmcnt(0)\n\ts_barrier" ::: "memory")

__host__ __device__ constexpr int rho16(int c) { return (c & 3) | (((c >> 3) & 1) << 2) | (((c >> 2) & 1) << 3); }
DI int bid_qg(int g, int j) { return (2 * (g >> 2) + (j >> 3)) + 8 * ((g & 3) + 4 * (j & 7)); }
DI int bid_att(int g, int j) { return (j >> 1) + 8 * ((j & 1) + 2 * g); }
DI int bid_w(int rb, int sl) { return (rb >> 2) + 8 * ((rb & 3) + 4 * sl); }

DI void transpose8x8(const v4u (&in)[8], v4u (&out)[8]) {
#pragma unroll
  for (int p = 0; p < 4; ++p)
#pragma unroll
    for (int q = 0; q < 4; ++q) {
      unsigned a = in[2 * q][p], b = in[2 * q + 1][p];
      out[2 * p][q] = __builtin_amdgcn_perm(b, a, 0x05040100u);
      out[2 * p + 1][q] = __builtin_amdgcn_perm(b, a, 0x07060302u);
    }
}

__global__ void __launch_bounds__(256) k_prep(const unsigned short* __restrict__ x, const unsigned short* __restrict__ wqkv,
                                              const unsigned short* __restrict__ wo, const unsigned short* __restrict__ wg,
                                              const unsigned short* __restrict__ wu, const unsigned short* __restrict__ wd,
                                              unsigned char* __restrict__ ws) {
  __shared__ float red[64][33];
  int b = blockIdx.x;
  const int t = threadIdx.x;
  {
    const int i = b * 256 + t;
    if (i < 10 * 256) *(unsigned*)(ws + CNT_OFF + i * 128) = 0u;
    if (i == 10 * 256) *(unsigned*)(ws + TMO_OFF) = 0u;
  }
  if (b >= 240) {
    const int mt = b - 240, tok = t & 31, part = t >> 5;
    const unsigned short* src = x + (size_t)(mt * 32 + tok) * D_ + part * 64;
    v4u v[8];
#pragma unroll
    for (int i = 0; i < 8; ++i) v[i] = *(const v4u*)(src + i * 8);
    unsigned char* dst = ws + X_OFF + (size_t)mt * 32 * 1024;
#pragma unroll
    for (int i = 0; i < 8; ++i) {
      const int ks = part * 4 + (i >> 1), h = i & 1;
      *(v4u*)(dst + ks * 1024 + (tok + 32 * h) * 16) = v[i];
    }
#pragma unroll
    for (int c = 0; c < 2; ++c) {
      float s = 0.f;
#pragma unroll
      for (int i = 0; i < 4; ++i)
#pragma unroll
        for (int d = 0; d < 4; ++d) s += bflo(v[c * 4 + i][d]) + bfhi(v[c * 4 + i][d]);
      const float m = s * (1.f / 32.f);
      float q = 0.f;
#pragma unroll
      for (int i = 0; i < 4; ++i)
#pragma unroll
        for (int d = 0; d < 4; ++d) {
          float a = bflo(v[c * 4 + i][d]) - m, e = bfhi(v[c * 4 + i][d]) - m;
          q += a * a + e * e;
        }
      *(f32x2*)(ws + ST_OFF + ((size_t)(mt * 32 + tok) * 16 + part * 2 + c) * 8) = f32x2{m, q};
    }
    return;
  }
  b = (b & ~15) | ((b & 7) << 1) | ((b >> 3) & 1);
  const unsigned short* W;
  int N, kind, slab, kbase = 0, nkb = 64;
  if (b < 48) { W = wqkv; N = 1536; kind = 0; slab = b; }
  else if (b < 104) { W = wg; N = F_; kind = 1; slab = b - 48; }
  else if (b < 160) { W = wu; N = F_; kind = 2; slab = b - 104; }
  else if (b < 176) { W = wo; N = D_; kind = 3; slab = b - 160; }
  else { W = wd; N = D_; kind = 4; slab = (b - 176) & 15; kbase = ((b - 176) >> 4) * 448; nkb = 56; }
  const int nb = t & 3, kb = t >> 2;
  const bool act = kb < nkb;
  const int n0 = slab * 32 + nb * 8, k0 = kbase + (act ? kb : 0) * 8;
  v4u in[8], out[8];
#pragma unroll
  for (int i = 0; i < 8; ++i) in[i] = *(const v4u*)(W + (size_t)(k0 + i) * N + n0);
  transpose8x8(in, out);
  const int ks = k0 >> 4, h = (k0 >> 3) & 1;
  int base, kstride, rb;
  if (kind == 0) {
    const int part = n0 >> 9, within = n0 & 511, j = within >> 5;
    base = WQ_OFF + j * WQ_SL + part * 1024; kstride = 3 * 1024; rb = within & 16;
  } else if (kind == 1 || kind == 2) {
    const int tt = n0 >> 4, j = tt / 7, i = tt % 7;
    base = WG_OFF + j * WG_SL + i * 1024; kstride = 7 * 1024; rb = (kind == 2) ? 16 : 0;
  } else if (kind == 3) {
    const int nt = n0 >> 5;
    base = WO_OFF + ((nt >> 1) * 64 + (nt & 1)) * 1024; kstride = 2048; rb = n0 & 16;
  } else {
    const int nt = n0 >> 5;
    base = WD_OFF + ((nt >> 1) * 224 + (nt & 1)) * 1024; kstride = 2048; rb = n0 & 16;
  }
  const int beta = (n0 >> 3) & 1;
  if (act) {
#pragma unroll
    for (int c = 0; c < 8; ++c) {
      const int rho = rb + (c & 3) + 4 * beta + 8 * (c >> 2);
      *(v4u*)(ws + base + ks * kstride + (rho + 32 * h) * 16) = out[c];
    }
  }
  if (kind <= 2) {
#pragma unroll
    for (int c = 0; c < 8; ++c) {
      float s = 0.f;
#pragma unroll
      for (int d = 0; d < 4; ++d) s += bflo(out[c][d]) + bfhi(out[c][d]);
      red[kb][nb * 8 + c] = s;
    }
    __syncthreads();
    if (kb < 16) {
      const int c = kb & 7, hh = kb >> 3;
      v4u o = {0u, 0u, 0u, 0u};
      if (hh == 0) {
        float s = 0.f;
#pragma unroll
        for (int i = 0; i < 64; ++i) s += red[i][nb * 8 + c];
        const float s0 = rbf(s), s1 = rbf(s - s0), s2 = rbf(s - s0 - s1);
        o[0] = pk2(s0, s1);
        o[1] = pk2(s2, 0.f);
      }
      const int rho = rb + (c & 3) + 4 * beta + 8 * (c >> 2);
      *(v4u*)(ws + base + 32 * kstride + (rho + 32 * hh) * 16) = o;
    }
  }
}

struct Ctx {
  rsrc_t rs;
  unsigned char* ws;
  LAS unsigned char* lds;
  int lane, w, h, l31, l16;
};

DI void refresh(Ctx& c) {
  int l = __builtin_amdgcn_mbcnt_hi(~0u, __builtin_amdgcn_mbcnt_lo(~0u, 0u));
  asm volatile("" : "+v"(l));
  c.lane = l; c.h = l >> 5; c.l31 = l & 31; c.l16 = l * 16;
}
#define STAMP(i) do {} while (0)

DI void wait_unit(const Ctx& c, int u, unsigned target) {
  if (c.w == 0) {
    gu32* p = (gu32*)(c.ws + CNT_OFF + (u * 256 + blockIdx.x) * 128);
    gu32* tmo = (gu32*)(c.ws + TMO_OFF);
#pragma clang loop unroll(disable)
    for (unsigned spins = 0;;) {
      if ((unsigned)__builtin_amdgcn_readfirstlane(__hip_atomic_load(p, RLX_AGENT)) >= target) break;
      ++spins;
      if (spins > (1u << 18)) { if (c.lane == 0) __hip_atomic_store(tmo, 1u, RLX_AGENT); break; }
      if ((spins & 1023u) == 0u && __builtin_amdgcn_readfirstlane(__hip_atomic_load(tmo, RLX_AGENT))) break;
    }
    __builtin_amdgcn_fence(__ATOMIC_ACQUIRE, "wavefront");
  }
  BAR_ALL();
}
DI void signal_unit(const Ctx& c, int u, int bid, int n) {
  BAR_ALL();
  if (c.w == 0 && c.lane < n) __hip_atomic_fetch_add((gu32*)(c.ws + CNT_OFF + (u * 256 + bid) * 128), 1u, RLX_AGENT);
}

DI void load_stats(const Ctx& c, int stoff, int tok, v4u (&sv)[4]) {
#pragma unroll
  for (int i = 0; i < 4; ++i) sv[i] = ldx(c.rs, tok * 128 + c.h * 64 + i * 16, stoff);
}
DI f32x2 row_stats(const v4u (&sv)[4], int h) {
  f32x4 v[4];
#pragma unroll
  for (int i = 0; i < 4; ++i) v[i] = __builtin_bit_cast(f32x4, sv[i]);
  float s = 0.f;
#pragma unroll
  for (int i = 0; i < 4; ++i) s += v[i][0] + v[i][2];
  s += swap32(s, h);
  const float mean = s * (1.f / 16.f);
  float q = 0.f;
#pragma unroll
  for (int i = 0; i < 4; ++i) {
    const float a = v[i][0] - mean, b = v[i][2] - mean;
    q += v[i][1] + v[i][3] + 32.f * (a * a + b * b);
  }
  q += swap32(q, h);
  const float var = rbf(q * (1.f / 512.f));
  const float sd = rbf(__builtin_amdgcn_sqrtf(rbf(var + 1e-5f)));
  return f32x2{rbf(mean), __builtin_amdgcn_rcpf(sd)};
}
DI v4u mean_frag(float mean, int h) {
  const unsigned nm = __builtin_bit_cast(unsigned, -mean) >> 16;
  v4u f = {h ? 0u : (nm | (nm << 16)), h ? 0u : nm, 0u, 0u};
  return f;
}
DI v4u pack8(const f32x16& a, int o, float s) {
  return v4u{pk2(a[o] * s, a[o + 1] * s), pk2(a[o + 2] * s, a[o + 3] * s), pk2(a[o + 4] * s, a[o + 5] * s), pk2(a[o + 6] * s, a[o + 7] * s)};
}
DI v4u ldsr(const Ctx& c, int off) { return *(const LAS v4u*)(c.lds + off + c.l16); }

DI void phase_qkv(const Ctx& c, int layer, int g, int j) {
  const int mt = 4 * g + c.w, xb = 2 * layer;
  const int xo = opq(X_OFF + xb * XF_BYTES + mt * 32 * 1024);
  v4u xa[32], sv[4];
#pragma unroll
  for (int ks = 0; ks < 32; ++ks) xa[ks] = ldx(c.rs, c.l16, xo + ks * 1024);
  load_stats(c, ST_OFF + xb * ST_BYTES + mt * 32 * 128, c.l31, sv);
  SB();
  f32x16 acc[3];
  v4u wa[4];
#pragma unroll
  for (int i = 0; i < 3; ++i) wa[i] = ldsr(c, i * 1024);
  f32x2 st;
  v4u xm;
#pragma unroll
  for (int ks = 0; ks < 33; ++ks)
#pragma unroll
  for (int i = 0; i < 3; ++i) {
    const int s = ks * 3 + i;
    if (s + 3 < 99) wa[(s + 3) & 3] = ldsr(c, (s + 3) * 1024);
    if (s == 90) { st = row_stats(sv, c.h); xm = mean_frag(st[0], c.h); }
    const v4u xf = ks < 32 ? xa[ks < 32 ? ks : 0] : xm;
    if (ks == 0) { if (i < 2) MF0(acc[i], "v", wa[s & 3], "v", xf); else MF0(acc[i], "v", xf, "v", wa[s & 3]); }
    else { if (i < 2) MF(acc[i], "v", wa[s & 3], "v", xf); else MF(acc[i], "v", xf, "v", wa[s & 3]); }
    SB();
  }
  NOP12();
  LAS float* tb = (LAS float*)(c.lds + L_MISC + c.w * 128);
  if (c.lane < 32) tb[c.lane] = st[1];
  const int qo = opq(layer * QKV_BYTES + (2 * j * 64 + mt) * 1024);
  stx(c.rs, pack8(acc[0], 0, st[1] * QSCALE), c.l16, Q_OFF + qo);
  stx(c.rs, pack8(acc[0], 8, st[1] * QSCALE), c.l16, Q_OFF + qo + 64 * 1024);
  stx(c.rs, pack8(acc[1], 0, st[1]), c.l16, K_OFF + qo);
  stx(c.rs, pack8(acc[1], 8, st[1]), c.l16, K_OFF + qo + 64 * 1024);
  const int vvo = (c.l31 >> 4) * 65536 + ((c.l31 & 15) + 16 * c.h) * 16;
#pragma unroll
  for (int s = 0; s < 2; ++s) {
    const f32x4 ra = *(const LAS f32x4*)(tb + 16 * s + 4 * c.h), rb = *(const LAS f32x4*)(tb + 16 * s + 8 + 4 * c.h);
    const int o = 8 * s;
    const v4u v = {pk2(acc[2][o] * ra[0], acc[2][o + 1] * ra[1]), pk2(acc[2][o + 2] * ra[2], acc[2][o + 3] * ra[3]),
                   pk2(acc[2][o + 4] * rb[0], acc[2][o + 5] * rb[1]), pk2(acc[2][o + 6] * rb[2], acc[2][o + 7] * rb[3])};
    stx(c.rs, v, vvo, V_OFF + qo + s * 512);
  }
}

DI float max16(float m, const f32x16& s) {
#pragma unroll
  for (int r = 0; r < 16; r += 2) m = __builtin_fmaxf(m, __builtin_fmaxf(s[r], s[r + 1]));
  return m;
}
#define PV(o, idx, pp) do { if ((idx) < NVA) MFN(o, "a", vf[idx], "v", pp); else MFN(o, "v", vf[idx], "v", pp); } while (0)
#define PV0(o, idx, pp) do { if ((idx) < NVA) MF0N(o, "a", vf[idx], "v", pp); else MF0N(o, "v", vf[idx], "v", pp); } while (0)
constexpr int NVA = 5;
DI v4u exp_pack(const f32x16& s, int o) {
  return v4u{pk2(__builtin_amdgcn_exp2f(s[o]), __builtin_amdgcn_exp2f(s[o + 1])), pk2(__builtin_amdgcn_exp2f(s[o + 2]), __builtin_amdgcn_exp2f(s[o + 3])),
             pk2(__builtin_amdgcn_exp2f(s[o + 4]), __builtin_amdgcn_exp2f(s[o + 5])), pk2(__builtin_amdgcn_exp2f(s[o + 6]), __builtin_amdgcn_exp2f(s[o + 7]))};
}
struct Masks { unsigned long long m[16]; };
DI void sel16(f32x16& t, const Masks& mk, float a, float b) {
#pragma unroll
  for (int r = 0; r < 16; ++r) { float x; asm volatile("v_cndmask_b32 %0, %1, %2, %3" : "=v"(x) : "v"(b), "v"(a), "s"(mk.m[r])); t[r] = x; }
  asm volatile("" : "+v"(t));
}
template <bool EDGE, int OFF>
DI v4u attn_qtile(const v4u (&kf)[10], const v4u (&vf)[20], v4u qf, int nneg, const Masks& mk, int h) {
  float m = NEGBIG;
  const float neg = NEGBIG, zer = 0.f;
  f32x16 o;
  if constexpr (EDGE) {
#pragma unroll
    for (int r = 0; r < 16; ++r) o[r] = 0.f;
#pragma unroll
    for (int u = 0; u < 9; ++u) {
      if (u >= nneg) {
        f32x16 s, cc;
        if (u == 0) { sel16(cc, mk, zer, neg); MFCN(s, "v", kf[OFF + u], "v", qf, cc); }
        else if (u == 8) { sel16(cc, mk, neg, zer); MFCN(s, "v", kf[OFF + u], "v", qf, cc); }
        else MF0(s, "v", kf[OFF + u], "v", qf);
        NOP12();
        m = max16(m, s);
      }
      SB();
    }
    m = __builtin_fmaxf(m, swap32(m, h));
    const float nm = -m;
#pragma unroll
    for (int u = 0; u < 9; ++u) {
      if (u >= nneg) {
        f32x16 cc, s;
        if (u == 0) sel16(cc, mk, nm, neg); else if (u == 8) sel16(cc, mk, neg, nm);
        else {
#pragma unroll
          for (int r = 0; r < 16; ++r) cc[r] = nm;
          asm volatile("" : "+v"(cc));
        }
        MFCN(s, "v", kf[OFF + u], "v", qf, cc);
        NOP12();
        const v4u p0 = exp_pack(s, 0);
        PV(o, 2 * (OFF + u), p0);
        const v4u p1 = exp_pack(s, 8);
        PV(o, 2 * (OFF + u) + 1, p1);
        NOP12();
      }
      SB();
    }
  } else {
    f32x16 sb[3], cm, ce;
    MF0(sb[1], "v", kf[OFF + 1], "v", qf);
    MF0(sb[2], "v", kf[OFF + 2], "v", qf);
    sel16(ce, mk, zer, neg);
    MFCN(sb[0], "v", kf[OFF], "v", qf, ce);
    sel16(cm, mk, neg, zer);
    asm volatile("" :: "v"(ce));
    SB();
#pragma unroll
    for (int u = 0; u < 9; ++u) {
      const int bi = (u + 1) % 3;
      m = max16(m, sb[bi]);
      asm volatile("" : "+v"(m));
      SB();
      if (u + 3 < 8) MF0(sb[bi], "v", kf[OFF + u + 3], "v", qf);
      if (u + 3 == 8) MFC(sb[bi], "v", kf[OFF + u + 3], "v", qf, cm);
      if (u == 6) asm volatile("s_nop 3" :: "v"(cm));
      SB();
    }
    m = __builtin_fmaxf(m, swap32(m, h));
    const float nm = -m;
#pragma unroll
    for (int r = 0; r < 16; ++r) cm[r] = nm;
    asm volatile("" : "+v"(cm));
    SB();
    MFCN(sb[1], "v", kf[OFF + 1], "v", qf, cm);
    sel16(ce, mk, nm, neg);
    MFCN(sb[0], "v", kf[OFF], "v", qf, ce);
    SB();
#pragma unroll
    for (int u = 0; u < 9; ++u) {
      const int t = u == 0 ? 1 : (u == 1 ? 0 : u), bi = (u + 1) & 1;
      const v4u p0 = exp_pack(sb[bi], 0);
      if (u == 0) PV0(o, 2 * (OFF + t), p0); else PV(o, 2 * (OFF + t), p0);
      if (u == 0 || u == 7) asm volatile("" :: "v"(ce));
      if (u == 6) asm volatile("" :: "v"(cm));
      SB();
      if (u == 5) sel16(ce, mk, neg, nm);
      const v4u p1 = exp_pack(sb[bi], 8);
      PV(o, 2 * (OFF + t) + 1, p1);
      if (u + 2 < 8) MFC(sb[bi], "v", kf[OFF + u + 2], "v", qf, cm);
      if (u + 2 == 8) MFC(sb[bi], "v", kf[OFF + u + 2], "v", qf, ce);
      SB();
    }
    NOP12();
  }
  return pack8(o, 0, __builtin_amdgcn_rcpf(o[8]));
}

template <typename PF>
DI void phase_attn(const Ctx& c, int layer, int g, int j, PF&& prefetch) {
  const int head = 2 * j + (c.w & 1), tb0 = 4 * g + 2 * (c.w >> 1);
  const int hb = layer * QKV_BYTES + head * 65536;
  v4u kf[10], vf[20];
  const v4u qf0 = ldx(c.rs, c.l16, Q_OFF + hb + tb0 * 1024), qf1 = ldx(c.rs, c.l16, Q_OFF + hb + (tb0 + 1) * 1024);
#pragma unroll
  for (int u = 0; u < 10; ++u) kf[u] = ldx(c.rs, c.l16, K_OFF + hb + max(tb0 - 8 + u, 0) * 1024);
  const unsigned ones = (c.l31 == 16 || c.l31 == 20) ? 0x3f803f80u : 0u;
#pragma unroll
  for (int u = 0; u < 20; ++u) vf[u] = v4u{ones, ones, ones, ones};
  if (c.l31 < 16) {
    const int vo = (c.l31 + 16 * c.h) * 16;
#pragma unroll
    for (int u = 0; u < 10; ++u) {
      const int tb = max(tb0 - 8 + u, 0);
      vf[2 * u] = ldx(c.rs, vo, V_OFF + hb + tb * 1024);
      vf[2 * u + 1] = ldx(c.rs, vo + 512, V_OFF + hb + tb * 1024);
    }
  }
  SB();
  prefetch();
  SB();
  Masks mk;
  const int td = c.l31 - 4 * c.h;
#pragma unroll
  for (int r = 0; r < 16; ++r) mk.m[r] = __builtin_amdgcn_ballot_w64((r & 3) + 8 * (r >> 2) > td);
  asm volatile("s_nop 1");
  SB();
  const int oo = O_OFF + layer * XF_BYTES + (tb0 * 32 + head) * 1024;
  if (g < 2) {
    stx(c.rs, attn_qtile<true, 0>(kf, vf, qf0, 8 - tb0, mk, c.h), c.l16, oo);
    stx(c.rs, attn_qtile<true, 1>(kf, vf, qf1, 7 - tb0, mk, c.h), c.l16, oo + 32 * 1024);
  } else {
    stx(c.rs, attn_qtile<false, 0>(kf, vf, qf0, 0, mk, c.h), c.l16, oo);
    stx(c.rs, attn_qtile<false, 1>(kf, vf, qf1, 0, mk, c.h), c.l16, oo + 32 * 1024);
  }
}

DI void red_put(const Ctx& c, int off, const f32x16& a) {
#pragma unroll
  for (int q = 0; q < 4; ++q) *(LAS f32x4*)(c.lds + off + q * 1024 + c.l16) = f32x4{a[4 * q], a[4 * q + 1], a[4 * q + 2], a[4 * q + 3]};
}
DI void red_add(const Ctx& c, int off, f32x16& a) {
#pragma unroll
  for (int q = 0; q < 4; ++q) {
    const f32x4 v = *(const LAS f32x4*)(c.lds + off + q * 1024 + c.l16);
    a[4 * q] += v[0]; a[4 * q + 1] += v[1]; a[4 * q + 2] += v[2]; a[4 * q + 3] += v[3];
  }
}
DI void ksplit_tail(const Ctx& c, f32x16 (&acc)[4], v4u (&res)[2], float (&y)[16]) {
  const int w = c.w;
  const bool mhi = (w >> 1) != 0, nhi = (w & 1) != 0;
  f32x16 k0, k1;
  if (mhi) { red_put(c, L_RED + w * 8192, acc[0]); red_put(c, L_RED + w * 8192 + 4096, acc[1]); k0 = acc[2]; k1 = acc[3]; }
  else { red_put(c, L_RED + w * 8192, acc[2]); red_put(c, L_RED + w * 8192 + 4096, acc[3]); k0 = acc[0]; k1 = acc[1]; }
  BAR_LGKM();
  const int pr = L_RED + (w ^ 2) * 8192;
  red_add(c, pr, k0);
  red_add(c, pr + 4096, k1);
  asm volatile("s_waitcnt lgkmcnt(0)" ::: "memory");
  f32x16 o;
  if (nhi) { red_put(c, pr, k0); o = k1; } else { red_put(c, pr, k1); o = k0; }
  BAR_LGKM();
  red_add(c, L_RED + (w ^ 3) * 8192, o);
#pragma unroll
  for (int d = 0; d < 4; ++d) {
    y[2 * d] = rbf(bflo(res[0][d]) + rbf(o[2 * d]));
    y[2 * d + 1] = rbf(bfhi(res[0][d]) + rbf(o[2 * d + 1]));
    y[8 + 2 * d] = rbf(bflo(res[1][d]) + rbf(o[8 + 2 * d]));
    y[8 + 2 * d + 1] = rbf(bfhi(res[1][d]) + rbf(o[8 + 2 * d + 1]));
  }
  res[0] = v4u{pk2(y[0], y[1]), pk2(y[2], y[3]), pk2(y[4], y[5]), pk2(y[6], y[7])};
  res[1] = v4u{pk2(y[8], y[9]), pk2(y[10], y[11]), pk2(y[12], y[13]), pk2(y[14], y[15])};
}
DI void publish_x(const Ctx& c, int xb, int omt, int ont, const v4u (&res)[2], const float (&y)[16]) {
  const int xo = X_OFF + xb * XF_BYTES + (omt * 32 + 2 * ont) * 1024;
  stx(c.rs, res[0], c.l16, xo);
  stx(c.rs, res[1], c.l16, xo + 1024);
  float s = 0.f;
#pragma unroll
  for (int r = 0; r < 16; ++r) s += y[r];
  s += swap32(s, c.h);
  const float m = s * (1.f / 32.f);
  float q = 0.f;
#pragma unroll
  for (int r = 0; r < 16; ++r) q += (y[r] - m) * (y[r] - m);
  q += swap32(q, c.h);
  if (c.h == 0) {
    const v2u sv = {__builtin_bit_cast(unsigned, m), __builtin_bit_cast(unsigned, q)};
    __builtin_amdgcn_raw_buffer_store_b64(sv, c.rs, c.l31 * 128, ST_OFF + xb * ST_BYTES + omt * 32 * 128 + ont * 8, SC1);
  }
}

__global__ void __launch_bounds__(256, 1) k_main(unsigned char* __restrict__ ws, unsigned short* __restrict__ dout) {
  extern __shared__ __attribute__((aligned(16))) unsigned char smem[];
  Ctx c;
  c.ws = ws;
  c.rs = __builtin_amdgcn_make_buffer_rsrc((void*)ws, 0, WS_END, 0x00020000);
  c.lds = (LAS unsigned char*)smem;
  c.lane = threadIdx.x & 63;
  c.w = __builtin_amdgcn_readfirstlane(threadIdx.x >> 6);
  c.h = c.lane >> 5;
  c.l31 = c.lane & 31;
  c.l16 = c.lane * 16;
  const int lds0 = (int)(unsigned)(uintptr_t)c.lds;
  int gq, jq, ja, ga, rb, sl, omt, ont;
  auto roles = [&]() {
    const int b = opq(blockIdx.x), bx = b & 7, bi = b >> 3;
    c.w = opq(c.w);
    gq = 4 * (bx >> 1) + (bi & 3); jq = 8 * (bx & 1) + (bi >> 2);
    ja = 2 * bx + (bi & 1); ga = bi >> 1;
    rb = 4 * bx + (bi & 3); sl = bi >> 2;
    omt = 2 * rb + (c.w >> 1); ont = 2 * sl + (c.w & 1);
    refresh(c);
  };
  roles();
#define ln15 (c.lane & 15)
#define w c.w
  STAMP(0);
  auto pf_wq = [&]() { const int p0 = (w == 3) ? 74 : 25 * w; dma_n<25>(c.rs, c.l16, WQ_OFF + jq * WQ_SL + p0 * 1024, lds0 + p0 * 1024); };
  pf_wq();
  v4u res[2];
  res[0] = ldw(c.rs, c.l16, X_OFF + (omt * 32 + 2 * ont) * 1024);
  res[1] = ldw(c.rs, c.l16, X_OFF + (omt * 32 + 2 * ont + 1) * 1024);
  v4u wd[56];
#define wdo (WD_OFF + (sl * 112 + 28 * w) * 2048)

#pragma unroll 1
  for (int layer = 0; layer < 2; ++layer) {
    const int u0 = layer * 5;
    if (layer == 0) BAR_ALL(); else wait_unit(c, u0 + 0, 16u);
    STAMP(1 + layer * 20);
    roles();
    phase_qkv(c, layer, gq, jq);
    roles();
    STAMP(2 + layer * 20);
    signal_unit(c, u0 + 1, bid_att(gq + ln15, jq), min(3, 16 - gq));
    STAMP(3 + layer * 20);
    if (w > 0) { const int p0 = (w == 3) ? 37 : 19 * (w - 1); dma_n<19>(c.rs, c.l16, WG_OFF + jq * WG_SL + p0 * 1024, lds0 + p0 * 1024); }
    wait_unit(c, u0 + 1, (unsigned)(min(ga, 2) + 1));
    STAMP(4 + layer * 20);
    {
      auto pf = [&]() {
        dma_n<14>(c.rs, c.l16, WG_OFF + jq * WG_SL + (56 + 14 * w) * 1024, lds0 + (56 + 14 * w) * 1024);
        if (layer == 0) {
          const int wdo1 = opq(wdo);
#pragma unroll
          for (int i = 0; i < 20; ++i) wd[i] = ldw(c.rs, c.l16, wdo1 + i * 1024);
        }
      };
      roles();
      phase_attn(c, layer, ga, ja, pf);
    }
    roles();
    STAMP(5 + layer * 20);
    signal_unit(c, u0 + 2, bid_w(2 * ga + (ln15 >> 3), ln15 & 7), 16);
    STAMP(6 + layer * 20);
    {
      v4u wo[16];
      const int woo = opq(WO_OFF + (sl * 32 + 8 * w) * 2048);
#pragma unroll
      for (int i = 0; i < 16; ++i) wo[i] = ldw(c.rs, c.l16, woo + i * 1024);
      SB();
      wait_unit(c, u0 + 2, 16u);
      STAMP(7 + layer * 20);
      const int ao = opq(O_OFF + layer * XF_BYTES + (2 * rb * 32 + 8 * w) * 1024);
      v4u xa[16];
#pragma unroll
      for (int k = 0; k < 8; ++k) { xa[2 * k] = ldx(c.rs, c.l16, ao + k * 1024); xa[2 * k + 1] = ldx(c.rs, c.l16, ao + (32 + k) * 1024); }
      SB();
      f32x16 acc[4];
#pragma unroll
      for (int k = 0; k < 8; ++k) {
        if (k == 0) {
          MF0(acc[0], "v", wo[0], "v", xa[0]); MF0(acc[1], "v", wo[1], "v", xa[0]); MF0(acc[2], "v", wo[0], "v", xa[1]); MF0(acc[3], "v", wo[1], "v", xa[1]);
        } else {
          MF(acc[0], "v", wo[2 * k], "v", xa[2 * k]); MF(acc[1], "v", wo[2 * k + 1], "v", xa[2 * k]);
          MF(acc[2], "v", wo[2 * k], "v", xa[2 * k + 1]); MF(acc[3], "v", wo[2 * k + 1], "v", xa[2 * k + 1]);
        }
        SB();
      }
      NOP12();
      float y[16];
      roles();
      ksplit_tail(c, acc, res, y);
      publish_x(c, 2 * layer + 1, omt, ont, res, y);
    }
    roles();
    STAMP(8 + layer * 20);
    signal_unit(c, u0 + 3, bid_qg(rb >> 1, ln15), 16);
    STAMP(9 + layer * 20);
    if (w > 0) dma_n<14>(c.rs, c.l16, WG_OFF + jq * WG_SL + (112 + 14 * (w - 1)) * 1024, lds0 + (112 + 14 * (w - 1)) * 1024);
    wait_unit(c, u0 + 3, 16u);
    STAMP(10 + layer * 20);
    {
      const int mt = 4 * gq + w, xb = 2 * layer + 1;
      const int xo = opq(X_OFF + xb * XF_BYTES + mt * 32 * 1024);
      constexpr int XW = 22, XA = 5;
      v4u xa[XW], sv[4];
#pragma unroll
      for (int ks = 0; ks < XW; ++ks) xa[ks] = ldx(c.rs, c.l16, xo + ks * 1024);
      load_stats(c, ST_OFF + xb * ST_BYTES + mt * 32 * 128, c.l31, sv);
      SB();
      f32x16 acc[7];
      v4u wa[4];
#pragma unroll
      for (int i = 0; i < 3; ++i) wa[i] = ldsr(c, i * 1024);
      f32x2 st;
      v4u xm;
      const int wgo = WG_OFF + jq * WG_SL;
#pragma clang loop unroll(full)
      for (int ks = 0; ks < 33; ++ks)
#pragma clang loop unroll(full)
      for (int i = 0; i < 7; ++i) {
        const int s = ks * 7 + i;
        if (s == 8 * 7) { BAR_LGKM(); dma_n<14>(c.rs, c.l16, wgo + (154 + 14 * w) * 1024, lds0 + (14 * w) * 1024); }
        if (s == 12 * 7) { BAR_LGKM(); dma_n<7>(c.rs, c.l16, wgo + (210 + 7 * w) * 1024, lds0 + (56 + 7 * w) * 1024); }
        if (s == 13 * 7 && layer == 0) {
          const int wdo1 = opq(wdo);
#pragma unroll
          for (int q = 20; q < 38; ++q) wd[q] = ldw(c.rs, c.l16, wdo1 + q * 1024);
        }
        if (s == 22 * 7 - 3) BAR_ALL();
        if (s == 22 * 7 && layer == 0) {
          const int wdo1 = opq(wdo);
#pragma unroll
          for (int q = 38; q < 56; ++q) wd[q] = ldw(c.rs, c.l16, wdo1 + q * 1024);
        }
        if (s + 3 < 231) { const int p = s + 3; wa[p & 3] = ldsr(c, (p < 154 ? p : p - 154) * 1024); }
        if (s == 210) { st = row_stats(sv, c.h); xm = mean_frag(st[0], c.h); }
        if (ks == 0) MF0(acc[i], "v", wa[s & 3], "a", xa[0]);
        else if (ks == 32) MF(acc[i], "v", wa[s & 3], "v", xm);
        else if (ks % XW < XA) MF(acc[i], "v", wa[s & 3], "a", xa[ks % XW]);
        else MF(acc[i], "v", wa[s & 3], "v", xa[ks % XW]);
        if (i == 6 && ks + XW < 32) xa[ks % XW] = ldx(c.rs, c.l16, xo + (ks + XW) * 1024);
        SB();
      }
      NOP12();
      STAMP(11 + layer * 20);
      const float c1 = -st[1] * LOG2E, c2 = st[1] * st[1];
      const int ho = opq(H_OFF + layer * HF_BYTES + (mt * 112 + 7 * jq) * 1024);
#pragma unroll
      for (int i = 0; i < 7; ++i) {
        float o[8];
#pragma unroll
        for (int r = 0; r < 8; ++r) {
          const float e = __builtin_amdgcn_exp2f(acc[i][r] * c1);
          o[r] = acc[i][r] * acc[i][8 + r] * c2 * __builtin_amdgcn_rcpf(1.0f + e);
        }
        stx(c.rs, v4u{pk2(o[0], o[1]), pk2(o[2], o[3]), pk2(o[4], o[5]), pk2(o[6], o[7])}, c.l16, ho + i * 1024);
      }
    }
    roles();
    STAMP(12 + layer * 20);
    signal_unit(c, u0 + 4, bid_w(2 * gq + (ln15 >> 3), ln15 & 7), 16);
    STAMP(13 + layer * 20);
    if (layer == 0 && w > 0) { const int p0 = 33 * (w - 1); dma_n<33>(c.rs, c.l16, WQ_OFF + jq * WQ_SL + p0 * 1024, lds0 + p0 * 1024); }
#pragma unroll
    for (int i = 0; i < 56; i += 8)
      asm volatile("" : "+a"(wd[i]), "+a"(wd[i + 1]), "+a"(wd[i + 2]), "+a"(wd[i + 3]), "+a"(wd[i + 4]), "+a"(wd[i + 5]), "+a"(wd[i + 6]), "+a"(wd[i + 7]));
    wait_unit(c, u0 + 4, 16u);
    STAMP(14 + layer * 20);
    {
      const int ao = opq(H_OFF + layer * HF_BYTES + (2 * rb * 112 + 28 * w) * 1024);
      constexpr int XW = 16;
      v4u xa[2 * XW];
#pragma unroll
      for (int k = 0; k < XW; ++k) { xa[2 * k] = ldx(c.rs, c.l16, ao + k * 1024); xa[2 * k + 1] = ldx(c.rs, c.l16, ao + (112 + k) * 1024); }
      SB();
      f32x16 acc[4];
#pragma unroll
      for (int k = 0; k < 28; ++k) {
        const int q = k % XW;
        if (k == 0) {
          MF0(acc[0], "a", wd[0], "v", xa[0]); MF0(acc[1], "a", wd[1], "v", xa[0]); MF0(acc[2], "a", wd[0], "v", xa[1]); MF0(acc[3], "a", wd[1], "v", xa[1]);
        } else {
          MF(acc[0], "a", wd[2 * k], "v", xa[2 * q]); MF(acc[1], "a", wd[2 * k + 1], "v", xa[2 * q]);
          MF(acc[2], "a", wd[2 * k], "v", xa[2 * q + 1]); MF(acc[3], "a", wd[2 * k + 1], "v", xa[2 * q + 1]);
        }
        if (k + XW < 28) { xa[2 * q] = ldx(c.rs, c.l16, ao + (k + XW) * 1024); xa[2 * q + 1] = ldx(c.rs, c.l16, ao + (112 + k + XW) * 1024); }
        SB();
      }
      NOP12();
      float y[16];
      roles();
      ksplit_tail(c, acc, res, y);
      if (layer == 0) {
        publish_x(c, 2, omt, ont, res, y);
        STAMP(15);
        signal_unit(c, 5, bid_qg(rb >> 1, ln15), 16);
        STAMP(16);
      } else {
        unsigned short* dp = dout + (size_t)(omt * 32 + c.l31) * D_ + ont * 32 + 8 * c.h;
        *(v4u*)dp = res[0];
        *(v4u*)(dp + 16) = res[1];
        STAMP(35);
      }
    }
  }
}

#undef w
extern "C" void kernel_launch(void* const* d_in, const int* in_sizes, int n_in, void* d_out, int out_size, void* d_ws, size_t ws_size,
                              hipStream_t stream) {
  const unsigned short* x = (const unsigned short*)d_in[0];
  const unsigned short* wqkv = (const unsigned short*)d_in[1];
  const unsigned short* wo = (const unsigned short*)d_in[2];
  const unsigned short* wg = (const unsigned short*)d_in[4];
  const unsigned short* wu = (const unsigned short*)d_in[5];
  const unsigned short* wd = (const unsigned short*)d_in[6];
  unsigned char* ws = (unsigned char*)d_ws;
  (void)hipFuncSetAttribute((const void*)k_main, hipFuncAttributeMaxDynamicSharedMemorySize, LDS_BYTES);
  k_prep<<<304, 256, 0, stream>>>(x, wqkv, wo, wg, wu, wd, ws);
  k_main<<<256, 256, LDS_BYTES, stream>>>(ws, (unsigned short*)d_out);
}
```

```cpp
#define UNR 0
#define CL1_ 352
#define WOF 1
#define DNF 1
#include <hip/hip_runtime.h>
#include <hip/hip_bf16.h>
#include <stdint.h>

#define STAMPS 0

#define GAS __attribute__((address_space(1)))
#define LAS __attribute__((address_space(3)))
typedef unsigned v4u __attribute__((ext_vector_type(4)));
typedef unsigned v2u __attribute__((ext_vector_type(2)));
typedef float f32x4 __attribute__((ext_vector_type(4)));
typedef float f32x2 __attribute__((ext_vector_type(2)));
typedef float f32x16 __attribute__((ext_vector_type(16)));
typedef short bf16x8 __attribute__((ext_vector_type(8)));
typedef __bf16 bf16x2_t __attribute__((ext_vector_type(2)));
typedef GAS unsigned gu32;
typedef __amdgpu_buffer_rsrc_t rsrc_t;
#define DI __device__ __forceinline__
#define RLX_AGENT __ATOMIC_RELAXED, __HIP_MEMORY_SCOPE_AGENT
#define SB() __builtin_amdgcn_sched_barrier(0)

constexpr int S_ = 2048, D_ = 512, F_ = 1792;
constexpr float LOG2E = 1.4426950408889634f;
constexpr float QSCALE = 0.25f * LOG2E;
constexpr float NEGBIG = -1.0e30f;
constexpr int SC1 = 16;
#define STW 4
#define CSW 1
#define WSC 16
#define WSCS " sc1"
constexpr int OOB = 0x7ff00000;

constexpr int NUNIT = 20;
constexpr int CNT_OFF = 0;
constexpr int SLOT_OFF = CNT_OFF + NUNIT * 256 * 128;
constexpr int WQ_OFF = SLOT_OFF + 4096;
constexpr int WQ_SL = 33 * 3 * 1024;
constexpr int WO_OFF = WQ_OFF + 16 * WQ_SL;
constexpr int WG_OFF = WO_OFF + 8 * 32 * 2 * 1024;
constexpr int WG_SL = 33 * 7 * 1024;
__host__ __device__ constexpr int wg_piece0(int i) { return i < 3 ? i : (i < 5 ? 99 + i - 3 : 165 + (i - 5) * 33); }
__host__ __device__ constexpr int wg_kstr(int i) { return i < 3 ? 3 : (i < 5 ? 2 : 1); }
__host__ __device__ constexpr int dn_ks(int kk) { return kk < 16 ? 7 * (kk >> 2) + (kk & 3) : (kk < 20 ? 7 * (kk - 16) + 4 : 7 * ((kk - 20) >> 1) + 5 + (kk & 1)); }
constexpr int WD_OFF = WG_OFF + 16 * WG_SL;
constexpr int XF_BYTES = S_ * D_ * 2;
constexpr int ST_BYTES = S_ * 16 * 8;
constexpr int X_OFF = WD_OFF + 8 * 112 * 2 * 1024 - XF_BYTES;
constexpr int ST_OFF = X_OFF + 4 * XF_BYTES - ST_BYTES;
constexpr int QKV_BYTES = 32 * 64 * 1024;
constexpr int Q_OFF = ST_OFF + 4 * ST_BYTES;
constexpr int K_OFF = Q_OFF + QKV_BYTES;
constexpr int V_OFF = K_OFF + 2 * QKV_BYTES;
constexpr int O_OFF = V_OFF + 2 * QKV_BYTES;
constexpr int HF_BYTES = S_ * F_ * 2;
constexpr int H_OFF = O_OFF + 2 * XF_BYTES;
constexpr int DBG_OFF = H_OFF + HF_BYTES;
constexpr int WS_END = DBG_OFF + 2 * 256 * 64 * 8;
constexpr int NTCH = 18;
static_assert(DBG_OFF <= 32 * NTCH * 65536, "translation warm-up covers the workspace");

constexpr int L_RED_WO = 112 * 1024, L_RED_DN = 26 * 1024;
constexpr int L_MISC = 154 * 1024, L_MA = 158 * 1024;
constexpr int LDS_BYTES = 160 * 1024;

DI float bflo(unsigned u) { return __builtin_bit_cast(float, u << 16); }
DI float bfhi(unsigned u) { return __builtin_bit_cast(float, u & 0xffff0000u); }
DI unsigned pk2(float lo, float hi) {
  bf16x2_t v;
  v[0] = (__bf16)lo;
  v[1] = (__bf16)hi;
  return __builtin_bit_cast(unsigned, v);
}
DI float rbf(float x) { return (float)(__bf16)x; }
DI f32x16 mfma32(v4u a, v4u b, f32x16 c) {
  return __builtin_amdgcn_mfma_f32_32x32x16_bf16(__builtin_bit_cast(bf16x8, a), __builtin_bit_cast(bf16x8, b), c, 0, 0, 0);
}
#define MFS "v_mfma_f32_32x32x16_bf16 "
#define MF(acc, CA, a, CB, b) asm volatile(MFS "%0, %1, %2, %0" : "+v"(acc) : CA(a), CB(b))
#define MFN(acc, CA, a, CB, b) asm volatile("s_nop 1\n\t" MFS "%0, %1, %2, %0" : "+v"(acc) : CA(a), CB(b))
#define MF0(acc, CA, a, CB, b) asm volatile(MFS "%0, %1, %2, 0" : "=&v"(acc) : CA(a), CB(b))
#define MF0N(acc, CA, a, CB, b) asm volatile("s_nop 1\n\t" MFS "%0, %1, %2, 0" : "=&v"(acc) : CA(a), CB(b))
#define MFC(d, CA, a, CB, b, c) asm volatile(MFS "%0, %1, %2, %3" : "=&v"(d) : CA(a), CB(b), "v"(c))
#define MFCN(d, CA, a, CB, b, c) asm volatile("s_nop 1\n\t" MFS "%0, %1, %2, %3" : "=&v"(d) : CA(a), CB(b), "v"(c))
#define MFW(acc, CA, a, CB, b, w1, w2, w3) asm volatile(MFS "%0, %1, %2, %0" : "+v"(acc) : CA(a), CB(b), "v"(w1), "v"(w2), "v"(w3))
#define MF0W(acc, CA, a, CB, b, w1, w2, w3) asm volatile(MFS "%0, %1, %2, 0" : "=&v"(acc) : CA(a), CB(b), "v"(w1), "v"(w2), "v"(w3))
#define NOP12() do { asm volatile("s_nop 11"); SB(); } while (0)
DI float swap32(float x, int h) {
  unsigned u = __builtin_bit_cast(unsigned, x);
  auto r = __builtin_amdgcn_permlane32_swap(u, u, false, false);
  return __builtin_bit_cast(float, h ? r[0] : r[1]);
}
DI v4u ldw(rsrc_t rs, int voff, int soff) { return __builtin_amdgcn_raw_buffer_load_b128(rs, voff, soff, WSC); }
DI v4u ldx(rsrc_t rs, int voff, int soff) { return __builtin_amdgcn_raw_buffer_load_b128(rs, voff, soff, SC1); }
DI void stx(rsrc_t rs, v4u v, int voff, int soff) {
  __builtin_amdgcn_raw_buffer_store_b128(v, rs, voff, soff, SC1);
  asm volatile("s_nop 1" :: "v"(v));
}
#define DMA_I(k) "buffer_load_dwordx4 %0, %1, %2 offen offset:" #k WSCS " lds"
template <int N> DI void dma(rsrc_t rs, int voff, int soff, int lds) {
  if constexpr (N == 4)
    asm volatile("s_mov_b32 m0, %3\n\ts_nop 0\n\t" DMA_I(0) "\n\t" DMA_I(1024) "\n\t" DMA_I(2048) "\n\t" DMA_I(3072) :: "v"(voff), "s"(rs), "s"(soff), "s"(lds) : "memory");
  else if constexpr (N == 3)
    asm volatile("s_mov_b32 m0, %3\n\ts_nop 0\n\t" DMA_I(0) "\n\t" DMA_I(1024) "\n\t" DMA_I(2048) :: "v"(voff), "s"(rs), "s"(soff), "s"(lds) : "memory");
  else if constexpr (N == 2)
    asm volatile("s_mov_b32 m0, %3\n\ts_nop 0\n\t" DMA_I(0) "\n\t" DMA_I(1024) :: "v"(voff), "s"(rs), "s"(soff), "s"(lds) : "memory");
  else
    asm volatile("s_mov_b32 m0, %3\n\ts_nop 0\n\t" DMA_I(0) :: "v"(voff), "s"(rs), "s"(soff), "s"(lds) : "memory");
}
struct DmaRun {
  int so;
  DI void begin(int soff, int lds) { so = soff; asm volatile("s_mov_b32 m0, %1" : "+s"(so) : "s"(lds)); }
  DI void piece(rsrc_t rs, int voff, int i) {
    if ((i & 3) == 3) asm volatile("buffer_load_dwordx4 %1, %2, %0 offen offset:3072" WSCS " lds\n\ts_add_u32 m0, m0, 0x1000\n\ts_add_u32 %0, %0, 0x1000" : "+s"(so) : "v"(voff), "s"(rs) : "memory", "scc");
    else asm volatile("buffer_load_dwordx4 %0, %1, %2 offen offset:%3" WSCS " lds" :: "v"(voff), "s"(rs), "s"(so), "n"((i & 3) * 1024) : "memory");
  }
};
DI int opq(int x);
template <int NP> DI void dma_n(rsrc_t rs, int voff, int soff, int lds) {
  soff = opq(soff);
  lds = opq(lds);
#pragma unroll
  for (int i = 0; i + 4 <= NP; i += 4) dma<4>(rs, voff, soff + i * 1024, lds + i * 1024);
  if constexpr (NP % 4 != 0) dma<NP % 4>(rs, voff, soff + (NP / 4) * 4096, lds + (NP / 4) * 4096);
}
DI int opq(int x) { x = __builtin_amdgcn_readfirstlane(x); asm volatile("" : "+s"(x)); return x; }
#define WAIT_VM0() asm volatile("s_waitcnt vmcnt(0)" ::: "memory")
#define BAR_LGKM() asm volatile("s_waitcnt lgkmcnt(0)\n\ts_barrier" ::: "memory")
#define BAR_ALL() asm volatile("s_waitcnt vmcnt(0) lgkmcnt(0)\n\ts_barrier" ::: "memory")

__host__ __device__ constexpr int rho16(int c) { return (c & 3) | (((c >> 3) & 1) << 2) | (((c >> 2) & 1) << 3); }
DI int bid_qg(int g, int j) { return (2 * (g >> 2) + (j >> 3)) + 8 * ((g & 3) + 4 * (j & 7)); }
DI int bid_att(int g, int j) { return (j >> 1) + 8 * ((j & 1) + 2 * g); }
DI int bid_w(int rb, int sl) { return (rb >> 2) + 8 * ((rb & 3) + 4 * sl); }

struct Ctx {
  rsrc_t rs;
  unsigned char* ws;
  LAS unsigned char* lds;
  int lane, w, h, l31, l16;
};

DI void refresh(Ctx& c) {
  int l = __builtin_amdgcn_mbcnt_hi(~0u, __builtin_amdgcn_mbcnt_lo(~0u, 0u));
  asm volatile("" : "+v"(l));
  c.lane = l; c.h = l >> 5; c.l31 = l & 31; c.l16 = l * 16;
}
#define STAMP(i) do {} while (0)

DI void spin(const Ctx& c, int u, unsigned target, unsigned have) {
  unsigned n = 0u, v;
  int off = CNT_OFF + (u * 256 + opq(blockIdx.x)) * 128;
  have = (unsigned)opq((int)have);
  target = (unsigned)opq((int)target);
  asm volatile("s_cmp_ge_u32 %1, %5\n\ts_cbranch_scc1 2f\n"
               "1:\n\tglobal_load_dword %2, %3, %4 sc1\n\ts_add_u32 %0, %0, 1\n\ts_waitcnt vmcnt(0)\n\tv_readfirstlane_b32 %1, %2\n\t"
               "s_cmp_ge_u32 %1, %5\n\ts_cbranch_scc1 2f\n\ts_cmp_lt_u32 %0, 0x20000\n\ts_cbranch_scc1 1b\n2:"
               : "+s"(n), "+s"(have), "=&v"(v) : "v"(off), "s"(c.ws), "s"(target) : "scc", "memory");
}
DI void wait_unit(const Ctx& c, int u, unsigned target) {
  spin(c, u, target, c.w == 0 ? 0u : ~0u);
  BAR_ALL();
}
DI unsigned poll_at(const Ctx& c, unsigned off) { asm volatile("" : "+v"(off)); return __hip_atomic_load((gu32*)(c.ws + off), RLX_AGENT); }
DI unsigned poll_issue(const Ctx& c, int u) { return poll_at(c, CNT_OFF + (u * 256 + opq(blockIdx.x)) * 128); }
DI void wave_wait(const Ctx& c, int u, unsigned target, unsigned first) {
  spin(c, u, target, first);
  SB();
}
struct Signal {
  int off, one;
  unsigned long long m;
  DI Signal(const Ctx& c, int u, int bid, int n) {
    off = CNT_OFF + (u * 256 + bid) * 128;
    one = 1;
    m = c.w == 0 ? (1ull << n) - 1ull : 0ull;
    const unsigned lo = (unsigned)opq((int)(unsigned)m);
    m = lo;
    asm volatile("" : "+v"(off), "+v"(one), "+s"(m));
  }
  DI void fire(const Ctx& c) const {
    asm volatile("s_mov_b64 exec, %3\n\tglobal_atomic_add %0, %1, %2\n\ts_mov_b64 exec, -1" :: "v"(off), "v"(one), "s"(c.ws), "s"(m) : "memory");
  }
};
DI void signal_unit(const Ctx& c, int u, int bid, int n) {
  const Signal sg(c, u, bid, n);
  BAR_ALL();
  sg.fire(c);
}

#define FSTAT 0
struct Chk {
  unsigned long long m;
  DI Chk() { m = ~0ull; asm volatile("" : "+s"(m)); }
  DI unsigned long long bad(float a, float b) const { return __builtin_amdgcn_ballot_w64(__builtin_isunordered(a, b)) & m; }
  DI void confirm(const Ctx& c, int u, unsigned target) {
    spin(c, u, target, 0u);
    m = 0ull;
    asm volatile("" : "+s"(m));
  }
};
#define EA 36
#define CLR(so_, off) asm volatile("buffer_store_dwordx4 %0, %1, %2, %3 offen offset:%4 sc1" :: "v"(nan4), "v"(c.l16), "s"(c.rs), "s"(so_), "n"(off) : "memory")
#define CLR16(so_) asm volatile("s_mov_b64 exec, 0xffff\n\tbuffer_store_dwordx4 %0, %1, %2, %3 offen sc1\n\ts_mov_b64 exec, -1" :: "v"(nan4), "v"(c.l16), "s"(c.rs), "s"(so_) : "memory")
DI v2u ld_stat(const Ctx& c, int stoff, int i) { return __builtin_amdgcn_raw_buffer_load_b64(c.rs, (STW == 4 ? (c.lane & 15) * 16 + (c.lane & 16) / 2 : c.l31 * 8) + c.h * 2048 + i * 256, stoff, SC1); }
struct StatSteps {
  float s, q, t, mean, rstd, sd;
  DI static float M(v2u v) { const unsigned t = v[0]; return __builtin_bit_cast(float, t); }
  DI static float Q(v2u v) { const unsigned t = v[1]; return __builtin_bit_cast(float, t); }
  DI void step(int n, const v2u (&sv)[8], int h) {
    if (n == 0) s = (M(sv[0]) + M(sv[1])) + (M(sv[2]) + M(sv[3]));
    if (n == 1) s += (M(sv[4]) + M(sv[5])) + (M(sv[6]) + M(sv[7]));
    if (n == 2) t = swap32(s, h);
    if (n == 3) q = (Q(sv[0]) + Q(sv[1])) + (Q(sv[2]) + Q(sv[3]));
    if (n == 4) q += (Q(sv[4]) + Q(sv[5])) + (Q(sv[6]) + Q(sv[7]));
    if (n == 5) { mean = (s + t) * (1.f / 512.f); s = mean * mean; }
    if (n == 6) t = swap32(q, h);
    if (n == 7) q = __builtin_elementwise_maximum(__builtin_fmaf(q + t, 1.f / 512.f, -s), 0.f);
    if (n == 8) { q = rbf(q); mean = rbf(mean); }
    if (n == 9) q = rbf(q + 1e-5f);
    if (n == 10) sd = __builtin_amdgcn_sqrtf(q);
    if (n == 11) sd = rbf(sd);
    if (n == 12) rstd = __builtin_amdgcn_rcpf(sd);
    if (n < 8) asm volatile("" : "+v"(s));
    if (n >= 2 && n < 8) asm volatile("" : "+v"(t));
    if (n >= 3 && n < 10) asm volatile("" : "+v"(q));
    if (n >= 5) asm volatile("" : "+v"(mean));
    if (n >= 10) asm volatile("" : "+v"(sd));
    if (n >= 12) asm volatile("" : "+v"(rstd));
  }
};
DI v4u mean_frag(float mean) {
  const unsigned nm = __builtin_bit_cast(unsigned, -mean) >> 16, n2 = nm | (nm << 16);
  v4u f = {n2, n2, n2, n2};
  return f;
}
DI v4u pack8(const f32x16& a, int o, float s) {
  return v4u{pk2(a[o] * s, a[o + 1] * s), pk2(a[o + 2] * s, a[o + 3] * s), pk2(a[o + 4] * s, a[o + 5] * s), pk2(a[o + 6] * s, a[o + 7] * s)};
}
struct LdsBases {
  int b0, b1, b2;
  DI LdsBases(const Ctx& c) {
    b0 = (int)(unsigned)(uintptr_t)c.lds + c.l16; b1 = b0 + 65536; b2 = b0 + 131072;
    asm volatile("" : "+v"(b0), "+v"(b1), "+v"(b2));
  }
};
DI v4u ldsr(const LdsBases& lb, int off) {
  const int b = off < 65536 ? lb.b0 : (off < 131072 ? lb.b1 : lb.b2);
  return *(const LAS v4u*)(uintptr_t)(unsigned)(b + (off & 65535));
}

constexpr int CODE_LINES0 = 0, CODE_LINES1 = CL1_ + 18 * STAMPS;
DI rsrc_t code_rsrc() {
  unsigned lo, hi;
  asm volatile("s_getpc_b64 s[40:41]\n\ts_add_u32 s40, s40, k_all@rel32@lo+4\n\ts_addc_u32 s41, s41, k_all@rel32@hi+12\n\t"
               "s_mov_b32 %0, s40\n\ts_mov_b32 %1, s41" : "=s"(lo), "=s"(hi) :: "s40", "s41", "scc");
  return __builtin_amdgcn_make_buffer_rsrc((void*)(((unsigned long long)hi << 32) | lo), 0, CODE_LINES1 * 128, 0x00020000);
}

DI void rstd_table(const Ctx& c, float rstd) {
  if (c.lane < 32) *(LAS float*)(c.lds + L_MISC + c.w * 128 + c.lane * 4) = rstd;
}
struct QkvEpi {
  int o0, o1, ov, vvo;
  f32x4 ra[2], rb[2];
  DI void init(const Ctx& c, int a, int b, int v) {
    o0 = opq(a); o1 = opq(b); ov = opq(v);
    vvo = (c.l31 >> 4) * 65536 + ((c.l31 & 15) + 16 * c.h) * 16;
    asm volatile("" : "+v"(vvo));
  }
  DI void table(const Ctx& c, int rt) {
    const LAS float* tb = (const LAS float*)(c.lds + rt + c.w * 128);
#pragma unroll
    for (int s = 0; s < 2; ++s) { ra[s] = *(const LAS f32x4*)(tb + 16 * s + 4 * c.h); rb[s] = *(const LAS f32x4*)(tb + 16 * s + 8 + 4 * c.h); }
  }
  DI void qk(const Ctx& c, const f32x16& a, int t, int hf, float s) { stx(c.rs, pack8(a, 8 * hf, s), c.l16, (t ? o1 : o0) + hf * 65536); }
  DI void v(const Ctx& c, const f32x16& a, int s) {
    const int o = 8 * s;
    const v4u u = {pk2(a[o] * ra[s][0], a[o + 1] * ra[s][1]), pk2(a[o + 2] * ra[s][2], a[o + 3] * ra[s][3]),
                   pk2(a[o + 4] * rb[s][0], a[o + 5] * rb[s][1]), pk2(a[o + 6] * rb[s][2], a[o + 7] * rb[s][3])};
    stx(c.rs, u, vvo, ov + s * 512);
  }
};
#define QKV_TAIL(w0, w1, w2, xl, c0, c1, c2, s0, s1) do { \
    MF(acc[0], "v", w0, "v", xl); MF(acc[0], "v", c0, "v", xm); SB(); \
    MF(acc[1], "v", w1, "v", xl); MF(acc[1], "v", c1, "v", xm); SB(); \
    asm volatile("s_nop 9"); SB(); \
    ep.qk(c, acc[0], 0, 0, s0); SB(); \
    MF(acc[2], "v", xl, "v", w2); SB(); \
    ep.qk(c, acc[0], 0, 1, s0); SB(); \
    MF(acc[2], "v", xm, "v", c2); SB(); \
    ep.qk(c, acc[1], 1, 0, s1); SB(); \
    ep.qk(c, acc[1], 1, 1, s1); SB(); \
    ep.v(c, acc[2], 0); ep.v(c, acc[2], 1); } while (0)
#define QKV_MF(ks, i, wf, xf) do { \
    if ((ks) == 0) { if ((i) < 2) MF0(acc[i], "v", wf, "v", xf); else MF0(acc[i], "v", xf, "v", wf); } \
    else { if ((i) < 2) MF(acc[i], "v", wf, "v", xf); else MF(acc[i], "v", xf, "v", wf); } } while (0)

#define QKV_MFW(ks, i, wf, xf, w1, w2, w3) do { \
    if ((ks) == 0) { if ((i) < 2) MF0W(acc[i], "v", wf, "v", xf, w1, w2, w3); else MF0W(acc[i], "v", xf, "v", wf, w1, w2, w3); } \
    else { if ((i) < 2) MFW(acc[i], "v", wf, "v", xf, w1, w2, w3); else MFW(acc[i], "v", xf, "v", wf, w1, w2, w3); } } while (0)

typedef short s16x4 __attribute__((ext_vector_type(4)));
DI v2u tr1(int a) { return __builtin_bit_cast(v2u, __builtin_amdgcn_ds_read_tr16_b64_v4i16((LAS s16x4*)(uintptr_t)(unsigned)a)); }
DI v4u trd(int base, int off, int d) {
  const v2u lo = tr1(base + off), hi = tr1(base + off + d);
  return v4u{lo[0], lo[1], hi[0], hi[1]};
}
DI int tr_lane(const Ctx& c, int rowb) {
  const int sw2 = ((c.lane & 1) << 1) | ((c.lane >> 1) & 1), q4 = (c.lane >> 2) & 3, g1 = (c.lane >> 4) & 1;
  return (8 * c.h + q4) * rowb + g1 * 32 + sw2 * 8;
}
#define WQ_SLOT(q) (((q) + 78) % 154)
DI unsigned long long phase_qkv(const Ctx& c, int layer, int g, int j, const Chk& ck) {
  const int mt = 4 * g + c.w, xb = 2 * layer;
  const int xo = opq(X_OFF + xb * XF_BYTES + mt * 32 * 1024);
  constexpr int XW = 10;
  v4u xa[32];
  v2u sv[8];
#pragma unroll
  for (int ks = 0; ks < XW; ++ks) xa[ks] = ldx(c.rs, c.l16, xo + ks * 1024);
  SB();
  const LdsBases lb(c);
  f32x16 acc[3];
  v4u wa[8];
#pragma unroll
  for (int i = 0; i < 7; ++i) wa[i] = ldsr(lb, WQ_SLOT(i) * 1024);
  v4u xm;
  StatSteps ss;
  QkvEpi ep;
  {
    const int qo = layer * QKV_BYTES + (2 * j * 64 + mt) * 1024;
    ep.init(c, Q_OFF + qo - layer * QKV_BYTES, K_OFF + qo, V_OFF + qo);
  }
#pragma clang loop unroll(full)
  for (int ks = 0; ks < 31; ++ks)
#pragma clang loop unroll(full)
  for (int i = 0; i < 3; ++i) {
    const int s = ks * 3 + i;
    if ((s & 3) == 0) QKV_MFW(ks, i, wa[s & 7], xa[ks], wa[(s + 1) & 7], wa[(s + 2) & 7], wa[(s + 3) & 7]); else QKV_MF(ks, i, wa[s & 7], xa[ks]);
    SB();
    if (s + 7 < 99) wa[(s + 7) & 7] = ldsr(lb, WQ_SLOT(s + 7) * 1024);
    SB();
    if (i == 0 && ks + XW < 32) xa[ks + XW] = ldx(c.rs, c.l16, xo + (ks + XW) * 1024);
    if (i > 0 && ks >= 12 && ks < 16) sv[2 * (ks - 12) + i - 1] = ld_stat(c, ST_OFF + xb * ST_BYTES + mt * 4096, 2 * (ks - 12) + i - 1);
    if (s >= 72 && s < 72 + 13) ss.step(s - 72, sv, c.h);
    if (s == 87) { xm = mean_frag(ss.mean); asm volatile("" : "+v"(xm)); }
    if (s == 88) rstd_table(c, ss.rstd);
    if (s == 92) ep.table(c, L_MISC);
    SB();
  }
  const float sq = ss.rstd * QSCALE;
  QKV_TAIL(wa[93 & 7], wa[94 & 7], wa[95 & 7], xa[31], wa[96 & 7], wa[97 & 7], wa[98 & 7], sq, ss.rstd);
  SB();
  return ck.bad(acc[1][0], ss.rstd);
}

DI float max16(float m, const f32x16& s) {
  asm volatile("v_max3_f32 %0, %0, %1, %2\n\tv_max3_f32 %0, %0, %3, %4\n\tv_max3_f32 %0, %0, %5, %6\n\tv_max3_f32 %0, %0, %7, %8\n\t"
      "v_max3_f32 %0, %0, %9, %10\n\tv_max3_f32 %0, %0, %11, %12\n\tv_max3_f32 %0, %0, %13, %14\n\tv_max3_f32 %0, %0, %15, %16"
      : "+v"(m)
      : "v"(s[0]), "v"(s[1]), "v"(s[2]), "v"(s[3]), "v"(s[4]), "v"(s[5]), "v"(s[6]), "v"(s[7]), "v"(s[8]), "v"(s[9]), "v"(s[10]), "v"(s[11]),
        "v"(s[12]), "v"(s[13]), "v"(s[14]), "v"(s[15]));
  return m;
}
#define PV(o, idx, pp) do { if ((idx) < NVA) MFN(o, "a", vf[idx], "v", pp); else MFN(o, "v", vf[idx], "v", pp); } while (0)
#define PVX(o, idx, pp) do { if ((idx) < NVA) MF(o, "a", vf[idx], "v", pp); else MF(o, "v", vf[idx], "v", pp); } while (0)
#define PVX0(o, idx, pp) do { if ((idx) < NVA) MF0(o, "a", vf[idx], "v", pp); else MF0(o, "v", vf[idx], "v", pp); } while (0)
#define KQCN(d, i, c) do { if ((i) < NKA) MFCN(d, "a", kf[i], "v", qf, c); else MFCN(d, "v", kf[i], "v", qf, c); } while (0)
#define KQC(d, i, c) do { if ((i) < NKA) MFC(d, "a", kf[i], "v", qf, c); else MFC(d, "v", kf[i], "v", qf, c); } while (0)
#define KQ0(d, i) do { if ((i) < NKA) MF0(d, "a", kf[i], "v", qf); else MF0(d, "v", kf[i], "v", qf); } while (0)
#define BUF(u) ((u) % 3 == 0 ? 1 : ((u) % 3 == 1 ? 0 : 2))
#define EX2(x) __builtin_amdgcn_exp2f(x)
#define PV0(o, idx, pp) do { if ((idx) < NVA) MF0N(o, "a", vf[idx], "v", pp); else MF0N(o, "v", vf[idx], "v", pp); } while (0)
#define VPRE 8
constexpr int NVA = 0, NKA = 7;
DI v4u exp_pack(const f32x16& s, int o) {
  return v4u{pk2(__builtin_amdgcn_exp2f(s[o]), __builtin_amdgcn_exp2f(s[o + 1])), pk2(__builtin_amdgcn_exp2f(s[o + 2]), __builtin_amdgcn_exp2f(s[o + 3])),
             pk2(__builtin_amdgcn_exp2f(s[o + 4]), __builtin_amdgcn_exp2f(s[o + 5])), pk2(__builtin_amdgcn_exp2f(s[o + 6]), __builtin_amdgcn_exp2f(s[o + 7]))};
}
struct Masks { unsigned long long m[16]; };
DI void sel16(f32x16& t, const Masks& mk, float a, float b) {
#pragma unroll
  for (int r = 0; r < 16; ++r) { float x; asm volatile("v_cndmask_b32 %0, %1, %2, %3" : "=v"(x) : "v"(b), "v"(a), "s"(mk.m[r])); t[r] = x; }
  asm volatile("" : "+v"(t));
}
template <int N = 0> DI void VMW(int n) {
  if (n == N) __builtin_amdgcn_s_waitcnt(0x0F70 | (N & 15) | ((N >> 4) << 14));
  else if constexpr (N < 32) VMW<N + 1>(n);
}
struct AttS {
  int kv[10], vo[5];
  rsrc_t rv;
  unsigned pm[8];
  f32x16 ce, cm;
  v4u vf[20];
};
DI void attn_setup(const Ctx& c, int layer, int g, int j, AttS& a) {
  const int head = 2 * j + (c.w & 1), tb0 = 4 * g + 2 * (c.w >> 1);
  const int hb = layer * QKV_BYTES + head * 65536;
#pragma unroll
  for (int u = 0; u < 10; ++u) { a.kv[u] = tb0 - 8 + u < 0 ? c.h * 512 : c.l16 + (tb0 - 8 + u) * 1024; asm volatile("" : "+v"(a.kv[u])); }
  const unsigned long long vb = (unsigned long long)(c.ws + V_OFF + hb);
  a.rv = __builtin_amdgcn_make_buffer_rsrc((void*)(((unsigned long long)(unsigned)opq((int)(vb >> 32)) << 32) | (unsigned)opq((int)vb)), 0, 65536, 0x00020000);
  const int td = c.l31 - 4 * c.h;
#pragma unroll
  for (int r = 0; r < 16; ++r) {
    const bool m = (r & 3) + 8 * (r >> 2) > td;
    a.ce[r] = m ? 0.f : NEGBIG; a.cm[r] = m ? NEGBIG : 0.f;
  }
  asm volatile("" : "+v"(a.ce), "+v"(a.cm));
#pragma unroll
  for (int p = 0; p < 8; ++p) { const float lo = a.cm[2 * p], hi = a.cm[2 * p + 1]; a.pm[p] = __builtin_amdgcn_perm(__builtin_bit_cast(unsigned, hi), __builtin_bit_cast(unsigned, lo), 0x0b0b0909u); }
  asm volatile("" : "+v"(a.ce), "+v"(a.cm), "+v"(a.pm[0]), "+v"(a.pm[1]), "+v"(a.pm[2]), "+v"(a.pm[3]), "+v"(a.pm[4]), "+v"(a.pm[5]), "+v"(a.pm[6]), "+v"(a.pm[7]));
  const unsigned ones = (c.l31 == 16 || c.l31 == 20) ? 0x3f803f80u : 0u;
#pragma unroll
  for (int p = 0; p < 5; ++p) {
    const unsigned o1 = tb0 - 8 + 2 * p >= 0 ? ones : 0u;
#pragma unroll
    for (int q = 0; q < 4; ++q) a.vf[4 * p + q] = v4u{o1, o1, o1, o1};
    a.vo[p] = (c.l31 + 16 * c.h) * 16 + (tb0 - 8 + 2 * p) * 1024;
    asm volatile("" : "+v"(a.vf[4 * p]), "+v"(a.vf[4 * p + 1]), "+v"(a.vf[4 * p + 2]), "+v"(a.vf[4 * p + 3]), "+v"(a.vo[p]));
  }
  SB();
}
#define VH0 2
#define VH1 6
#define VH2 10
#define VP 8
#define DS 8
template <bool B> struct BoolC { static constexpr bool value = B; };
#define CL0 9
#define CL1 14
#define CL2 22
#define CL3 26
#define CL4 30
static_assert(CL0 >= DS && CL0 < CL1 && CL1 < 19 && CL1 < CL2 && CL2 < CL3 && CL3 < CL4 && CL4 < DS + 27 && (CL1 - DS) % 4 != 0, "");
__host__ __device__ constexpr int p1_t(int n) { return n == 17 ? 1 : (n == 0 ? 0 : 1 - (n & 1)); }
__host__ __device__ constexpr int p1_k(int n) { return (n + 1) >> 1; }
template <bool L0, int NPW, typename PF, typename PW>
DI void phase_attn(const Ctx& c, int qo, int ko, int oo, AttS& a, PF&& pf, PW&& pw) {
  v4u kf[10];
  v4u (&vf)[20] = a.vf;
  const int (&vo)[5] = a.vo;
  const rsrc_t rv = a.rv;
  v4u qf[2];
#define LDA(d, C, vo_, so_, off) asm volatile("buffer_load_dwordx4 %0, %1, %2, %3 offen offset:%4 sc1" : C(d) : "v"(vo_), "s"(c.rs), "s"(so_), "n"(off) : "memory")
#define LDK(u) do { if ((u) < NKA) LDA(kf[u], "=a", a.kv[u], ko, 0); else LDA(kf[u], "=v", a.kv[u], ko, 0); } while (0)
  SB();
  LDA(qf[0], "=v", c.l16, qo, 0);
  LDK(0);
  LDA(qf[1], "=v", c.l16, qo, 1024);
#pragma unroll
  for (int u = 1; u < 10; ++u) LDK(u);
  auto ldv = [&](int n0, int n1) {
    asm volatile("s_mov_b32 exec_lo, 0xffff\n\ts_mov_b32 exec_hi, 0xffff");
#pragma unroll
    for (int u = n0; u < n1; ++u) asm volatile("buffer_load_dwordx4 %0, %1, %2, 0 offen offset:%3 sc1" : "+v"(vf[u]) : "v"(vo[u >> 2]), "s"(rv), "n"((u & 3) * 512) : "memory");
    asm volatile("s_mov_b64 exec, -1");
  };
  ldv(0, VP);
  SB();
  f32x16 sb[3], cm, o;
#define QK0(d, i, t) do { if ((i) < NKA) MF0(d, "a", kf[i], "v", qf[t]); else MF0(d, "v", kf[i], "v", qf[t]); } while (0)
#define QKC(d, i, t, cc) do { if ((i) < NKA) MFC(d, "a", kf[i], "v", qf[t], cc); else MFC(d, "v", kf[i], "v", qf[t], cc); } while (0)
  auto s1 = [&](int n, int it) {
    const int t = p1_t(n), k = p1_k(n);
    if (n == 0 || (n & 1)) { VMW(11 - (n == 0 ? 1 : k + 2) + VP + 4 * ((VP <= 8 && VH0 < it) + (VP <= 12 && VH1 < it) + (VP <= 16 && VH2 < it))); SB(); }
    if (k == t) QKC(sb[n % 3], k, t, a.ce); else if (k == t + 8) QKC(sb[n % 3], k, t, a.cm); else QK0(sb[n % 3], k, t);
  };
  s1(0, -1); s1(1, -1); s1(2, -1);
  asm volatile("s_nop 9");
  SB();
  float m[2] = {NEGBIG, NEGBIG};
#pragma unroll
  for (int n = 0; n < 16; ++n) {
    m[p1_t(n)] = max16(m[p1_t(n)], sb[n % 3]);
    SB();
    if (n + 3 < 18) s1(n + 3, n);
    if (n == VH0 && VP <= 8) ldv(8, 12);
    if (n == VH1 && VP <= 12) ldv(12, 16);
    if (n == VH2 && VP <= 16) ldv(16, 20);
    if (n == 11) pf(-2);
    if (n == 13) pf(-1);
    SB();
  }
#define NEGMAX(r, x, p) asm volatile("v_max_f32 %0, %1, %2\n\tv_sub_f32 %0, 0, %0" : "=&v"(r) : "v"(p[0]), "v"(p[1]))
#define SPLAT(x) do { _Pragma("unroll") for (int r = 0; r < 16; ++r) { float y; asm volatile("v_mov_b32 %0, %1" : "=v"(y) : "v"(x)); cm[r] = y; } asm volatile("" : "+v"(cm)); } while (0)
  float nm0, nm1;
  {
    unsigned x0 = __builtin_bit_cast(unsigned, m[0]), x1 = x0;
    asm volatile("" : "+v"(x0), "+v"(x1));
    SB();
    m[1] = max16(m[1], sb[1]);
    SB();
    asm volatile("v_permlane32_swap_b32 %0, %1\n\ts_nop 0\n\tv_max_f32 %2, %0, %1\n\tv_sub_f32 %2, 0, %2" : "+v"(x0), "+v"(x1), "=v"(nm0) : "v"(a.ce), "v"(a.cm));
    SPLAT(nm0);
    SB();
    QKC(sb[0], 0, 0, cm);
    SB();
    m[1] = max16(m[1], sb[2]);
    SB();
    QKC(sb[1], 1, 0, cm);
    unsigned y0 = __builtin_bit_cast(unsigned, m[1]), y1 = y0;
    asm volatile("s_nop 1\n\tv_permlane32_swap_b32 %0, %1\n\ts_nop 0\n\tv_max_f32 %2, %0, %1\n\tv_sub_f32 %2, 0, %2" : "+v"(y0), "+v"(y1), "=v"(nm1));
    SB();
  }
  v4u pp;
#pragma clang loop unroll(full)
  for (int hh = 0; hh < 36; ++hh) {
    const int pos = hh >> 1, u = pos % 9, o8 = (hh & 1) * 8;
    const f32x16& sc = sb[pos % 3];
    float e0 = EX2(sc[o8]), e1 = EX2(sc[o8 + 1]);
    asm volatile("" : "+v"(e0), "+v"(e1));
    SB();
    if (hh == 19) { asm volatile("" : "+v"(o)); SB(); stx(c.rs, pack8(o, 0, __builtin_amdgcn_rcpf(o[8])), c.l16, oo); SB(); }
    if (hh > 0) {
      const int q = hh - 1, pi = 2 * (q / 18) + q % 18;
      if (hh == 1) asm volatile("s_waitcnt vmcnt(%0)" :: "n"(21 + L0 - DS));
      if (q % 18 == 0) MF0(o, "v", vf[pi], "v", pp); else MF(o, "v", vf[pi], "v", pp);
    }
    SB();
    float e2 = EX2(sc[o8 + 2]), e3 = EX2(sc[o8 + 3]), e4 = EX2(sc[o8 + 4]), e5 = EX2(sc[o8 + 5]);
    asm volatile("" : "+v"(e2), "+v"(e3), "+v"(e4), "+v"(e5));
    SB();
    if (o8 == 0 && pos + 2 < 18) QKC(sb[(pos + 2) % 3], (pos + 2) / 9 + (pos + 2) % 9, (pos + 2) / 9, cm);
    if (hh == DS - 1) pf(-3);
    if (L0) { if (hh >= DS && hh < DS + 28) { if (((hh - DS) & 1) && hh - DS < NPW) pw((hh - DS) >> 1); pf(hh - DS); } }
    else if (hh >= DS && hh < DS + 28 && ((hh - DS) & 3) == 0) pf(100 + ((hh - DS) >> 2));
    pf(200 + hh);
    if (hh == 13) { asm volatile("" :: "v"(cm)); SPLAT(nm1); }
    if (hh == 31) asm volatile("" :: "v"(cm));
    SB();
    const float e6 = EX2(sc[o8 + 6]), e7 = EX2(sc[o8 + 7]);
    pp = v4u{pk2(e0, e1), pk2(e2, e3), pk2(e4, e5), pk2(e6, e7)};
    if (u == 0) { pp[0] &= a.pm[o8 / 2]; pp[1] &= a.pm[o8 / 2 + 1]; pp[2] &= a.pm[o8 / 2 + 2]; pp[3] &= a.pm[o8 / 2 + 3]; }
    if (u == 8) { pp[0] &= ~a.pm[o8 / 2]; pp[1] &= ~a.pm[o8 / 2 + 1]; pp[2] &= ~a.pm[o8 / 2 + 2]; pp[3] &= ~a.pm[o8 / 2 + 3]; }
    asm volatile("" : "+v"(pp));
    SB();
  }
  MFN(o, "v", vf[19], "v", pp);
  NOP12();
  stx(c.rs, pack8(o, 0, __builtin_amdgcn_rcpf(o[8])), c.l16, oo + 32 * 1024);
}

constexpr int NWS = 6, NXS = 5;
constexpr int A_X = NWS * 12288, A_XW = NXS * 4096, A_CS = A_X + 4 * A_XW, A_MT = A_CS + 3072, A_RT = A_MT + 512;
static_assert(A_RT + 2048 <= LDS_BYTES, "");
DI unsigned split2(float s) { const float s0 = rbf(s); return pk2(s0, s - s0); }
#define MF16(acc, a, b) asm volatile("v_mfma_f32_16x16x32_bf16 %0, %1, %2, %0" : "+v"(acc) : "v"(a), "v"(b))
#define SUMK(acc, f) MF16(acc, sel, f)
DI float csel(float a, float b, unsigned long long m) { float x; asm volatile("v_cndmask_b32 %0, %1, %2, %3" : "=v"(x) : "v"(a), "v"(b), "s"(m)); return x; }
enum { P_X, P_W, P_J, P_Z, P_MA, P_CP0, P_SA, P_TOUCH, P_CP1, P_BL, P_G };
#define JNT 0
#define JPOL ""
#define JS0 79
#define JSS 2
#define JSN 4
struct P0Op { signed char k, e, i, nx; short st; };
struct P0Plan { P0Op op[112]; short first[192], young[8]; int n, n0, n1; };
__host__ __device__ constexpr int p0_bstep(int e) { return e == 0 ? 0 : 24 * e - 14; }
__host__ __device__ constexpr bool p0_counted(int k) { return k != P_MA && k != P_SA; }
__host__ __device__ constexpr bool p0_slot(int p) { const int r = (p >> 1) % 12; return (p & 1) ? r >= 8 && p < 168 : r >= 1 && r <= 4; }
#define P0_INIT 3
#define P0_JA 6
__host__ __device__ constexpr int p0_xs(int e) { return e <= P0_JA ? e % NXS : (e + 2) % NXS; }
__host__ __device__ constexpr int p0_js(int e) { return (P0_JA + 1 + e) % NXS; }
__host__ __device__ constexpr P0Plan p0_build() {
  P0Plan L{};
  int n = 0, cur = -1;
  auto add = [&](int k, int e, int cnt, int earliest) {
    if (cur < earliest) cur = earliest;
    for (int i = 0; i < cnt; ++i) {
      while (cur >= 0 && !p0_slot(cur)) ++cur;
      L.op[n++] = P0Op{(signed char)k, (signed char)e, (signed char)i, -1, (short)cur};
      if (cur >= 0) ++cur;
    }
  };
  auto put = [&](int k, int e, int st) {
    int q = n++;
    while (q > 0 && L.op[q - 1].st > st) { L.op[q] = L.op[q - 1]; --q; }
    L.op[q] = P0Op{(signed char)k, (signed char)e, 0, -1, (short)st};
  };
  add(P_CP0, 0, 1, -1);
  add(P_Z, 0, 1, -1);
  for (int e = 0; e < 8; ++e) {
    if (e == P0_INIT) cur = 0;
    add(P_X, e, 4, e < NXS ? -1 : p0_bstep(p0_xs(e) + 1));
    add(P_W, e, 3, e < NWS ? -1 : p0_bstep(e - NWS + 2));
    if (e == 0) { add(P_BL, 0, 1, -1); add(P_MA, 0, 1, -1); L.n0 = n; }
    if (e == 1) L.n1 = n;
    if (e == P0_JA) { add(P_J, 0, 4, p0_bstep(p0_js(0) + 1)); add(P_J, 1, 4, p0_bstep(p0_js(1) + 1)); }
  }
  add(P_TOUCH, 0, 1, 0);
  add(P_CP1, 0, 1, 0);
  put(P_SA, 0, 2 * 48);
  for (int q = 0; q < JSN; ++q) put(P_G, q, 2 * (JS0 + JSS * q));
  L.n = n;
  for (int q = n - 1, nx = -1; q >= 0; --q) { L.op[q].nx = (signed char)nx; if (L.op[q].k <= P_J) nx = q; }
  int k = 0;
  for (int p = -1; p < 191; ++p) { while (k < n && L.op[k].st < p) ++k; L.first[p + 1] = (short)k; }
  for (int e = 0; e < 8; ++e) {
    int last = -1;
    for (int q = 0; q < n; ++q) if ((L.op[q].k == P_X || L.op[q].k == P_W) && L.op[q].e == e) last = q;
    int y = 0;
    for (int q = last + 1; q < L.first[p0_bstep(e) + 1]; ++q) y += p0_counted(L.op[q].k);
    L.young[e] = (short)(last < L.first[p0_bstep(e) + 1] ? y : -1);
  }
  return L;
}
constexpr P0Plan P0 = p0_build();
__host__ __device__ constexpr int p0_first(int p) { return P0.first[p + 1]; }
__host__ __device__ constexpr int p0_young(int e) { return P0.young[e]; }
__host__ __device__ constexpr bool p0_ok() {
  for (int e = 0; e < 8; ++e) if (p0_young(e) < 0 || p0_young(e) > 60) return false;
  for (int k = 1; k < P0.n; ++k) if (P0.op[k].st < P0.op[k - 1].st) return false;
  return P0.first[186] == P0.n && P0_JA >= 4 && P0_JA <= 7;
}
static_assert(p0_ok(), "plan of phase 0");
static_assert(JSN == 0 || (P0_JA < 7 && JS0 - 2 >= 12 * (P0_JA + 1) - 7), "the job block has landed when it is read");
DI void go_wait(const Ctx& c, unsigned have, unsigned p) {
  unsigned n = 0u, t, v, u;
  const int b_ = opq(blockIdx.x);
  int og = CNT_OFF + b_ * 128, os = SLOT_OFF + b_ * 4, one = 1;
  have = (unsigned)opq((int)have);
  p = (unsigned)opq((int)p);
  asm volatile("s_cmp_lg_u32 %1, 0\n\ts_cbranch_scc1 2f\n"
               "1:\n\tglobal_load_dword %3, %5, %8 sc1\n\tglobal_load_dword %4, %6, %8 sc1\n\ts_add_u32 %0, %0, 1\n\ts_waitcnt vmcnt(0)\n\t"
               "v_readfirstlane_b32 %1, %3\n\tv_readfirstlane_b32 %2, %4\n\ts_cmp_lg_u32 %1, 0\n\ts_cbranch_scc1 2f\n\t"
               "s_sub_u32 %2, %2, %9\n\ts_cmp_lg_u32 %2, 2\n\ts_cbranch_scc1 3f\n\t"
               "s_mov_b64 exec, 1\n\tglobal_atomic_add %6, %7, %8\n\ts_mov_b64 exec, -1\n"
               "3:\n\ts_cmp_lt_u32 %0, 0x20000\n\ts_cbranch_scc1 1b\n2:"
               : "+s"(n), "+s"(have), "=&s"(t), "=&v"(v), "=&v"(u) : "v"(og), "v"(os), "v"(one), "s"(c.ws), "s"(p) : "scc", "memory");
}
DI void spin_f(const Ctx& c, int u, unsigned tgt, unsigned have, unsigned p, unsigned& sent, unsigned all) {
  unsigned n = 0u, t, v, a;
  const int b4 = opq(opq(blockIdx.x) * 4);
  int off = CNT_OFF + u * 32768 + b4 * 32;
  have = (unsigned)opq((int)have);
  tgt = (unsigned)opq((int)tgt);
  p = (unsigned)opq((int)p);
  asm volatile("s_sub_u32 %2, %1, %8\n\ts_cmp_ge_i32 %2, 0\n\ts_cbranch_scc1 2f\n"
               "1:\n\tglobal_load_dword %4, %6, %7 sc1\n\ts_add_u32 %0, %0, 1\n\ts_waitcnt vmcnt(0)\n\tv_readfirstlane_b32 %1, %4\n\t"
               "s_sub_u32 %2, %1, %8\n\ts_cmp_ge_i32 %2, 0\n\ts_cbranch_scc1 2f\n\ts_cmp_lt_u32 %0, 16\n\ts_cbranch_scc1 1b\n\t"
               "s_lshl_b32 %2, %10, 5\n\ts_add_u32 %2, %2, %11\n\tv_mov_b32 %5, %2\n\tglobal_load_dword %4, %5, %7 sc1\n\ts_waitcnt vmcnt(0)\n\tv_readfirstlane_b32 %2, %4\n\t"
               "s_cmp_eq_u32 %2, 0\n\ts_cbranch_scc1 4f\n\t"
               "s_cmp_lg_u32 %3, 0\n\ts_cbranch_scc1 3f\n\ts_and_b32 %2, %10, 60\n\ts_lshl_b32 %2, %2, 5\n\ts_add_u32 %2, %2, %12\n\tv_mov_b32 %5, %2\n\tv_mov_b32 %4, 1\n\t"
               "s_mov_b64 exec, 1\n\tglobal_atomic_add %5, %4, %7\n\ts_mov_b64 exec, -1\n\ts_mov_b32 %3, 1\n\ts_waitcnt vmcnt(0)\n"
               "3:\n\tv_mbcnt_lo_u32_b32 %5, -1, 0\n\tv_and_b32 %5, 15, %5\n\tv_lshlrev_b32 %5, 7, %5\n\tv_add_u32 %5, %12, %5\n\tglobal_load_dword %4, %5, %7 sc1\n\ts_waitcnt vmcnt(0)\n\t"
               "v_cmp_gt_u32 vcc, %14, %4\n\ts_cbranch_vccz 2f\n\ts_branch 5f\n"
               "4:\n\ts_cmp_lg_u32 %3, 0\n\ts_cbranch_scc1 5f\n\ts_add_u32 %2, %10, %13\n\tv_mov_b32 %5, %2\n\tglobal_load_dword %4, %5, %7 sc1\n\ts_waitcnt vmcnt(0)\n\t"
               "v_readfirstlane_b32 %2, %4\n\tv_mov_b32 %4, 1\n\ts_sub_u32 %2, %2, %9\n\ts_cmp_lg_u32 %2, 2\n\ts_cbranch_scc1 5f\n\t"
               "s_mov_b64 exec, 1\n\tglobal_atomic_add %5, %4, %7\n\ts_mov_b64 exec, -1\n\ts_waitcnt vmcnt(0)\n"
               "5:\n\ts_cmp_lt_u32 %0, 0x10000\n\ts_cbranch_scc1 1b\n2:"
               : "+s"(n), "+s"(have), "=&s"(t), "+s"(sent), "=&v"(v), "=&v"(a)
               : "v"(off), "s"(c.ws), "s"(tgt), "s"(p), "s"(b4), "n"(CNT_OFF), "n"(CNT_OFF + 14 * 256 * 128), "n"(SLOT_OFF), "s"(opq((int)all)) : "scc", "vcc", "memory");
  SB();
}
DI void phase0(Ctx& c, unsigned char* ws, const unsigned short* __restrict__ x, const unsigned short* __restrict__ wqkv, const unsigned short* __restrict__ wo,
               const unsigned short* __restrict__ wg, const unsigned short* __restrict__ wu, const unsigned short* __restrict__ wd, v4u (&res)[2], unsigned& bl, AttS& at) {
  const int lds0 = (int)(unsigned)(uintptr_t)c.lds;
  const int b = blockIdx.x, bx = b & 7, bi = b >> 3, w = c.w;
  const rsrc_t rc = code_rsrc();
  unsigned pf1 = 0u;
  {
#define NOCP0 0
    const int vcp0 = !NOCP0 && (unsigned)(threadIdx.x - 192 + bi * 64) < 128u && bi < 2 ? (threadIdx.x - 192 + bi * 64) * 128 : OOB;
    asm volatile("buffer_load_dword %0, %1, %2, 0 offen" : "+v"(pf1) : "v"(vcp0), "s"(rc));
    SB();
  }
  c.ws = ws;
  c.rs = __builtin_amdgcn_make_buffer_rsrc((void*)ws, 0, WS_END, 0x00020000);
  STAMP(58);
  const int gq = 4 * (bx >> 1) + (bi & 3), jq = 8 * (bx & 1) + (bi >> 2), mt = 4 * gq + w;
  const rsrc_t rx = __builtin_amdgcn_make_buffer_rsrc((void*)x, 0, S_ * D_ * 2, 0x00020000);
  const rsrc_t rq = __builtin_amdgcn_make_buffer_rsrc((void*)wqkv, 0, D_ * 1536 * 2, 0x00020000);
  int qv[3];
  qv[0] = (c.lane >> 3) * 3072 + ((c.lane & 7) ^ ((c.lane >> 2) & 4)) * 16 + (jq & 1) * 1024 + (jq >> 1) * 128;
  qv[1] = qv[0] + 8 * 3072 - 1024;
  qv[2] = (c.lane >> 2) * 3072 + (c.lane & 3) * 16 + jq * 64;
#define ROTV 3
  const int rot = opq(2 * (bi & 3) + (ROTV == 0 ? bi >> 2 : (ROTV == 1 ? 0 : (ROTV == 2 ? bi & 4 : (ROTV == 3 ? 2 * (bi >> 2) : (ROTV == 4 ? (bi >> 2) & 1 : (bi >> 2) & 3))))));
  int xv[4];
  xv[0] = (c.lane >> 3) * 1024 + ((c.lane & 7) ^ (c.lane >> 4)) * 16; xv[1] = (xv[0] ^ 64) + 7168; xv[2] = xv[0] + 2 * 7168; xv[3] = xv[1] + 2 * 7168;
  const int lx = opq(lds0 + A_X + w * A_XW), lw = opq(lds0 + w * 3072);
  int sxo[8], swo[8];
  unsigned pf0 = 0u, pf2 = 0u, ma[4] = {1u, 1u, 1u, 1u};
  int vz, vbl, vma, vcp1, vsa, vto, jv, js, jd;
  rsrc_t rj;
  int vwq[2], swq[2][3];
  int vb[2], vs[2], vc = OOB;
  v4u g[8], sel;
  f32x4 js_[2] = {{0.f, 0.f, 0.f, 0.f}, {0.f, 0.f, 0.f, 0.f}};
  const unsigned xm_ = (unsigned)opq((b == 0 && w == 2) ? -1 : 0);
  unsigned long long xma = ((unsigned long long)xm_ << 32) | xm_, xsa = (unsigned)opq(w == 0 ? 0x8000 : 0);
  bl = 1u;
  auto vop = [&](int k) {
    const int kd = P0.op[k].k, e = P0.op[k].e, i = P0.op[k].i;
    if (kd == P_X) asm volatile("buffer_load_dwordx4 %0, %1, %2 offen offset:%3 lds" :: "v"(xv[i]), "s"(rx), "s"(sxo[e]), "n"(i * 1024) : "memory");
    if (kd == P_W) asm volatile("buffer_load_dwordx4 %0, %1, %2 offen offset:%3 lds" :: "v"(qv[i]), "s"(rq), "s"(swo[e]), "n"(i * 1024) : "memory");
    if (kd == P_J) {
      if (i < 3) asm volatile("buffer_load_dwordx4 %1, %2, %0 offen offset:%4" JPOL " lds\n\ts_add_u32 %0, %0, %3" : "+s"(js) : "v"(jv), "s"(rj), "s"(jd), "n"(i * 1024) : "memory", "scc");
      else asm volatile("buffer_load_dwordx4 %1, %2, %0 offen offset:3072" JPOL " lds\n\ts_add_u32 %0, %0, %3\n\ts_add_u32 %0, %0, 0x1000" : "+s"(js) : "v"(jv), "s"(rj), "s"(jd) : "memory", "scc");
    }
    if ((kd <= P_J || kd == P_Z) && P0.op[k].nx >= 0 && P0.op[P0.op[k].nx].i == 0) {
      const P0Op nx = P0.op[P0.op[k].nx];
      asm volatile("s_add_u32 m0, %0, %1" :: "s"(nx.k == P_W ? lw : lx), "n"(nx.k == P_W ? (nx.e % NWS) * 12288 : (nx.k == P_X ? p0_xs(nx.e) : p0_js(nx.e)) * 4096) : "scc");
      if (nx.st < 0) asm volatile("s_nop 0");
    }
    if (kd == P_Z) asm volatile("buffer_store_dword %0, %1, %2, 0 offen sc1" :: "v"(pf0), "v"(vz), "s"(c.rs) : "memory");
    if (kd == P_MA)
      asm volatile("s_mov_b64 exec, %6\n\tglobal_atomic_add %0, %4, %0, %5 sc0\n\tglobal_atomic_add %1, %4, %1, %5 offset:4 sc0\n\tglobal_atomic_add %2, %4, %2, %5 offset:8 sc0\n\t"
                   "global_atomic_add %3, %4, %3, %5 offset:12 sc0\n\ts_mov_b64 exec, -1" : "+v"(ma[0]), "+v"(ma[1]), "+v"(ma[2]), "+v"(ma[3]) : "v"(vma), "s"(c.ws), "s"(xma) : "memory");
    if (kd == P_SA) asm volatile("s_mov_b64 exec, %3\n\tglobal_atomic_add %0, %1, %0, %2 sc0\n\ts_mov_b64 exec, -1" : "+v"(bl) : "v"(vsa), "s"(c.ws), "s"(xsa) : "memory");
    if (kd == P_BL) asm volatile("s_mov_b64 exec, 0x7fff\n\tbuffer_load_dword %0, %1, %2, 0 offen sc1\n\ts_mov_b64 exec, -1" : "+v"(bl) : "v"(vbl), "s"(c.rs) : "memory");
    if (kd == P_CP1) asm volatile("buffer_load_dword %0, %1, %2, 0 offen" : "+v"(pf1) : "v"(vcp1), "s"(rc));
    if (kd == P_TOUCH) asm volatile("buffer_load_dword %0, %1, %2, 0 offen sc1" : "+v"(pf2) : "v"(vto), "s"(c.rs));
    if (kd == P_G) { SUMK(js_[e & 1], g[e]); stx(c.rs, g[e], vb[e & 1] + (e >> 1) * vs[e & 1], 0); }
    SB();
  };
  sxo[0] = opq(mt * 32768 + (rot & 7) * 128); swo[0] = opq((4 * (rot & 7) + w) * 49152);
  vbl = CNT_OFF + (c.lane * 256 + b) * 128;
  vz = (w == 0 && c.lane < NUNIT && c.lane != 1 && c.lane != 12) ? vbl : OOB;
  asm volatile("" : "+v"(vz), "+v"(vbl), "+v"(pf0), "+v"(bl));
#pragma unroll
  for (int k = 1; k < P0.n0; ++k) {
    if (P0.op[k].k == P_MA) { vma = SLOT_OFF + c.l16; asm volatile("" : "+s"(xma), "+v"(vma), "+v"(ma[0]), "+v"(ma[1]), "+v"(ma[2]), "+v"(ma[3])); }
    vop(k);
  }
  asm volatile("" : "+s"(xsa), "+v"(pf2));
#pragma unroll
  for (int e = 1; e < 8; ++e) { const int eg = (e + rot) & 7; sxo[e] = opq(mt * 32768 + eg * 128); swo[e] = opq((4 * eg + w) * 49152); }
  const int s01 = opq(WQ_OFF + (jq & 14) * WQ_SL + (jq & 1) * 1024), s2 = opq(WQ_OFF + jq * WQ_SL + 2048);
#pragma unroll
  for (int e = 6; e < 8; ++e) {
    const int ko = (4 * ((e + rot) & 7) + w) * 3072;
    swq[e - 6][0] = opq(s01 + ko); swq[e - 6][1] = opq(s01 + WQ_SL + ko); swq[e - 6][2] = opq(s2 + ko);
  }
  vwq[0] = w == (bx >> 1) ? c.l16 : OOB; vwq[1] = w == ((bx >> 1) ^ 2) ? c.l16 : OOB;
  asm volatile("" : "+v"(vwq[0]), "+v"(vwq[1]));
#define NOPF 0
  vcp1 = w == 3 && !(NOPF & 2) ? (2 + (bi & 7)) * 8192 + c.lane * 128 : OOB;
  vsa = SLOT_OFF + b * 4;
  vto = (w == 1 && c.lane < NTCH && !(NOPF & 1)) ? (bi * NTCH + c.lane) * 65536 : OOB;
  const int pb = opq((b + 255) & 255);
  const int kind = opq(pb < 56 ? 1 : (pb < 112 ? 2 : (pb < 128 ? 3 : 4)));
  const int slab = opq(pb < 128 ? (pb - (kind - 1) * 56) >> 1 : pb & 7), kc = opq(pb < 128 ? pb & 1 : (pb - 128) >> 3);
  {
    const int N2 = kind <= 2 ? 2 * F_ : 2 * D_;
    const unsigned long long W = (unsigned long long)(kind == 1 ? wg : (kind == 2 ? wu : (kind == 3 ? wo : wd)));
    const unsigned long long Wu = ((unsigned long long)(unsigned)opq((int)(W >> 32)) << 32) | (unsigned)opq((int)W);
    rj = __builtin_amdgcn_make_buffer_rsrc((void*)Wu, 0, opq(pb < 184 ? (kind == 3 ? D_ * D_ * 2 : D_ * F_ * 2) : 0), 0x00020000);
    jv = (c.lane >> 3) * N2 + (c.lane & 7) * 16;
    js = opq((kc * 256 + w * 64) * N2 + slab * 128);
    jd = opq(8 * N2 - 1024);
    asm volatile("" : "+v"(jv), "+v"(vcp1), "+v"(vsa), "+v"(vto));
  }
  int tw_[4][3], xsr[4], xs0, xs1;
#pragma unroll
  for (int k = 0; k < 4; ++k) {
    const int p = (k + w) & 3;
    tw_[k][0] = lds0 + p * 3072 + tr_lane(c, 128) + ((c.lane >> 3) & 1) * 64; tw_[k][1] = tw_[k][0] ^ 64; tw_[k][2] = lds0 + p * 3072 + 2048 + tr_lane(c, 64);
    xsr[k] = lx + c.l31 * 128 + ((2 * p + c.h) ^ ((c.l31 >> 1) & 7)) * 16;
    asm volatile("" : "+v"(tw_[k][0]), "+v"(tw_[k][1]), "+v"(tw_[k][2]), "+v"(xsr[k]));
  }
  int jb = lx + p0_js(0) * 4096 + tr_lane(c, 128);
  xs0 = lx + (c.lane & 15) * 128 + ((c.lane >> 4) ^ ((c.lane >> 1) & 7)) * 16; xs1 = xs0 ^ 64;
  const unsigned s1_ = ((c.lane & 3) == 0 && ((c.lane >> 2) & 1) == ((c.lane >> 4) & 1)) ? 0x3f803f80u : 0u;
  sel = v4u{s1_, s1_, s1_, s1_};
  f32x4 cs[3] = {{0.f, 0.f, 0.f, 0.f}, {0.f, 0.f, 0.f, 0.f}, {0.f, 0.f, 0.f, 0.f}};
  asm volatile("" : "+v"(jb), "+v"(xs0), "+v"(xs1), "+v"(sel), "+v"(cs[0]), "+v"(cs[1]), "+v"(cs[2]), "+v"(js_[0]), "+v"(js_[1]));
  auto wrd = [&](int s) {
    const int ks = s / 3, t = s % 3;
    return trd(tw_[ks & 3][t], ((ks >> 2) % NWS) * 12288, t < 2 ? 512 : 256);
  };
  f32x16 acc[3];
  constexpr int WD_ = 7, WR = 8;
  v4u xf[8], sf[4], wa[WR], cf[3], xm;
  float rstd, mean;
  QkvEpi ep;
  v4u ones = {0x3f803f80u, 0x3f803f80u, 0x3f803f80u, 0x3f803f80u};
  f32x4 sx[2] = {{0.f, 0.f, 0.f, 0.f}, {0.f, 0.f, 0.f, 0.f}}, gr[2] = {{0.f, 0.f, 0.f, 0.f}, {0.f, 0.f, 0.f, 0.f}};
  asm volatile("" : "+v"(ones), "+v"(sx[0]), "+v"(sx[1]), "+v"(gr[0]), "+v"(gr[1]));
  auto xrd = [&](int e, int k) { xf[(4 * e + k) & 7] = *(const LAS v4u*)(uintptr_t)(unsigned)(xsr[k] + p0_xs(e) * 4096); };
  auto srd = [&](int e, int k) { sf[k] = *(const LAS v4u*)(uintptr_t)(unsigned)(((k & 2) ? xs1 : xs0) + p0_xs(e) * 4096 + (k & 1) * 2048); };
  v4u wqf[6];
  {
    const int o01 = ((jq & 1) ? K_OFF : Q_OFF) + (2 * (jq & 14) * 64 + mt) * 1024;
    ep.init(c, o01, o01 + 128 * 1024, V_OFF + (2 * jq * 64 + mt) * 1024);
  }
  const unsigned long long m1 = __builtin_amdgcn_ballot_w64((c.lane & 1) != 0), m2 = __builtin_amdgcn_ballot_w64((c.lane & 2) != 0);
  int tw = ((c.lane >> 4) == ((c.lane >> 2) & 3)) ? A_MT + w * 128 + (c.lane & 15) * 4 : A_RT + 512 + c.lane * 4;
  int cw = lds0 + A_CS + c.l16 + w * 4, cr = lds0 + A_CS + c.l16, mr = lds0 + A_MT + w * 128 + c.l31 * 4, tr_ = lds0 + A_RT + w * 128 + 16 * c.h;
  unsigned bt = c.lane == 1 ? 2 * min(bi >> 1, 2) + 2 : (c.lane == 12 ? 4 * (((7 * jq + 6) >> 2) - ((7 * jq) >> 2)) + 13 : 0);
  tw += lds0;
  asm volatile("" : "+v"(cr), "+v"(mr), "+v"(tr_), "+v"(bt));
  const Signal sg(c, 1, bid_att(gq + ((c.lane & 15) >> 1), jq ^ (c.lane & 1)), 2 * min(3, 16 - gq));
  asm volatile("" : "+v"(tw), "+v"(cw));
  float va[2], me[2];
  SB();
#pragma unroll
  for (int k = P0.n0; k < P0.n1; ++k) vop(k);
  {
    const int ks0 = kc * 16 + 4 * w;
#pragma unroll
    for (int ct = 0; ct < 2; ++ct) {
      const int tt = 4 * slab + 2 * ct + (c.l31 >> 4), tq = (tt * 147) >> 10, tl = tt - 7 * tq;
      const int sg_ = wg_kstr(tl) * 1024, bg = WG_OFF + tq * WG_SL + wg_piece0(tl) * 1024 + (kind == 2 ? 256 : 0) + (c.l31 & 15) * 16;
      if (ct == c.h) vc = kind <= 2 && (!CSW || w == 3) ? bg + 32 * sg_ + kc * 512 + (CSW ? 0 : w * 4) : OOB;
      const int bw = (pb >= 184 ? OOB : (kind == 3 ? WO_OFF + slab * 65536 : WD_OFF + slab * 229376)) + ks0 * 2048 + ct * 1024 + c.l16;
      vs[ct] = kind <= 2 ? sg_ : 2048;
      vb[ct] = kind <= 2 ? bg + ks0 * sg_ + c.h * 512 : bw;
    }
    asm volatile("" : "+v"(vb[0]), "+v"(vb[1]), "+v"(vs[0]), "+v"(vs[1]), "+v"(vc));
  }
  SB();
#pragma unroll
  for (int k = P0.n1; k < p0_first(0); ++k) vop(k);
  STAMP(59);
  asm volatile("s_waitcnt vmcnt(%0)\n\ts_barrier" :: "n"(p0_young(0)) : "memory");
  STAMP(60);
  xrd(0, 0);
#pragma unroll
  for (int s = 0; s < WD_; ++s) { wa[s] = wrd(s); if (s < 4) srd(0, s); if (s < 3) xrd(0, s + 1); }
  SB();
#pragma clang loop unroll(full)
  for (int ks = 0; ks < 31; ++ks)
#pragma clang loop unroll(full)
  for (int t = 0; t < 3; ++t) {
    const int s = 3 * ks + t, e = s / 12, r = s % 12;
    QKV_MF(ks, t, wa[s % WR], xf[ks & 7]);
    SB();
    if (r == 5 && e < 7) asm volatile("s_waitcnt vmcnt(%0) lgkmcnt(8)\n\ts_barrier" :: "n"(p0_young(e + 1)) : "memory");
    if (s == 78) STAMP(61);
    if (s == 89) mean = *(const LAS float*)(uintptr_t)(unsigned)mr;
    if (s == 90) {
      asm volatile("s_waitcnt lgkmcnt(0)\n\ts_barrier" ::: "memory");
      cf[0] = *(const LAS v4u*)(uintptr_t)(unsigned)cr;
      cf[1] = *(const LAS v4u*)(uintptr_t)(unsigned)(cr + 1024);
      rstd = *(const LAS float*)(uintptr_t)(unsigned)(mr + 512);
    }
    if (s == 91) {
      cf[2] = *(const LAS v4u*)(uintptr_t)(unsigned)(cr + 2048);
#pragma unroll
      for (int q = 0; q < 2; ++q) { ep.ra[q] = *(const LAS f32x4*)(uintptr_t)(unsigned)(tr_ + 64 * q); ep.rb[q] = *(const LAS f32x4*)(uintptr_t)(unsigned)(tr_ + 64 * q + 32); }
    }
    if (s + WD_ < 96) wa[(s + WD_) % WR] = wrd(s + WD_);
    if (r >= 5 && r < 9 && e < 7) srd(e + 1, r - 5);
    if (r >= 9 && e < 7) xrd(e + 1, r - 9);
    if (r == 0 && e > 0) xrd(e, 3);
    if (s + 2 >= JS0 && s + 2 < JS0 + JSS * JSN && (s + 2 - JS0) % JSS == 0) { const int q = (s + 2 - JS0) / JSS; g[q] = trd(jb, (q >> 1) * 2048 + (q & 1) * 64, 512); }
    SB();
#pragma unroll
    for (int k = p0_first(2 * s); k < p0_first(2 * s + 1); ++k) vop(k);
    if (s >= 84 && s < 89) {
#pragma unroll
      for (int T = 0; T < 2; ++T) {
        const int n = s - 84 - T;
        if (n == 0) {
          asm volatile("" : "+v"(gr[T]), "+v"(sx[T]));
          va[T] = csel(csel(gr[T][0], gr[T][1], m1), csel(gr[T][2], gr[T][3], m1), m2);
          me[T] = sx[T][0] * (1.f / 512.f);
        }
        if (n == 1) va[T] = rbf(__builtin_fmaxf(va[T] * (1.f / 512.f) - me[T] * me[T], 0.f));
        if (n == 2) va[T] = __builtin_amdgcn_sqrtf(rbf(va[T] + 1e-5f));
        if (n == 3) {
          *(LAS float*)(uintptr_t)(unsigned)(tw + T * 64) = rbf(me[T]);
          *(LAS float*)(uintptr_t)(unsigned)(tw + T * 64 + 512) = __builtin_amdgcn_rcpf(rbf(va[T]));
        }
        if (n >= 0 && n < 3) asm volatile("" : "+v"(va[T]), "+v"(me[T]));
      }
      SB();
    }
    if (r < 3) SUMK(cs[r], wa[s % WR]);
    if (r < 3 && e >= 6) wqf[3 * (e - 6) + r] = wa[s % WR];
    if ((s >= 1 && s < 5) || (r >= 8 && e < 7)) {
      const int k4 = s < 5 ? s - 1 : r - 8;
      MF16(gr[k4 & 1], sf[k4], sf[k4]);
      SB();
#pragma unroll
      for (int k = p0_first(2 * s + 1); k < p0_first(2 * s + 2); ++k) vop(k);
      MF16(sx[k4 & 1], ones, sf[k4]);
    }
    SB();
    if (s >= 87 && s < 90) {
      asm volatile("" : "+v"(cs[s - 87]));
      const unsigned u = split2(cs[s - 87][0]);
      *(LAS unsigned*)(uintptr_t)(unsigned)(cw + (s - 87) * 1024) = c.h ? 0u : u;
    }
    SB();
  }
  xm = mean_frag(mean);
  const float sc = (jq & 1) ? rstd : rstd * QSCALE;
  asm volatile("" : "+v"(xm));
  STAMP(62);
  QKV_TAIL(wa[93 % WR], wa[94 % WR], wa[95 % WR], xf[7], cf[0], cf[1], cf[2], sc, sc);
  STAMP(63);
  attn_setup(c, 0, bi >> 1, 2 * bx + (bi & 1), at);
  asm volatile("s_waitcnt vmcnt(6)" : "+v"(pf1), "+v"(pf2), "+v"(bl), "+v"(ma[0]), "+v"(ma[1]), "+v"(ma[2]), "+v"(ma[3]) :: "memory");
  SB();
#pragma unroll
  for (int q = JSN; q < 8; ++q) g[q] = trd(jb, (q >> 1) * 2048 + (q & 1) * 64, 512);
  const v4u csv = *(const LAS v4u*)(uintptr_t)(unsigned)(cr + w * 1024);
  SB();
  if (w == 2) *(LAS v4u*)(c.lds + L_MA + c.l16) = v4u{ma[0], ma[1], ma[2], ma[3]};
  bl += bt;
  SB();
#pragma unroll
  for (int q = JSN; q < 8; ++q) {
    SUMK(js_[q & 1], g[q]);
    stx(c.rs, g[q], vb[q & 1] + (q >> 1) * vs[q & 1], 0);
    SB();
  }
#define NPRE 0
#pragma unroll
  for (int q = 0; q < NPRE; ++q) stx(c.rs, wqf[q], vwq[q / 3], swq[q / 3][q % 3]);
  asm volatile("s_nop 7" : "+v"(js_[0]), "+v"(js_[1]));
  *(LAS unsigned*)(c.lds + c.l16 + w * 4) = split2(c.h ? js_[1][0] : js_[0][0]);
  SB();
  asm volatile("s_waitcnt vmcnt(%0) lgkmcnt(0)\n\ts_barrier" :: "n"(8 - JSN + NPRE) : "memory");
  sg.fire(c);
  SB();
#pragma unroll
  for (int q = NPRE; q < 6; ++q) stx(c.rs, wqf[q], vwq[q / 3], swq[q / 3][q % 3]);
  stx(c.rs, *(const LAS v4u*)(c.lds + c.l16), vc, 0);
  stx(c.rs, csv, (w < 3 && gq == 0) ? c.l16 : OOB, opq((w == 2 ? s2 : s01 + w * WQ_SL) + 96 * 1024));
  SB();
  const int vre = ((2 * (4 * bx + (bi & 3)) + (w >> 1)) * 32 + c.l31) * 1024 + (2 * (bi >> 2) + (w & 1)) * 64 + 16 * c.h;
  res[0] = __builtin_amdgcn_raw_buffer_load_b128(rx, vre, 0, 0);
  res[1] = __builtin_amdgcn_raw_buffer_load_b128(rx, vre + 32, 0, 0);
}

DI float dot2bf(unsigned a, unsigned b, float acc) {
  return __builtin_amdgcn_fdot2_f32_bf16(__builtin_bit_cast(bf16x2_t, a), __builtin_bit_cast(bf16x2_t, b), acc, false);
}
struct KTail {
  int wa[3], ra, xo, so, sv;
  DI void init(const Ctx& c, int L_RED, int xb, int omt, int ont) {
    const int l0 = (int)(unsigned)(uintptr_t)c.lds + L_RED + c.l16;
#pragma unroll
    for (int i = 1; i < 4; ++i) wa[i - 1] = l0 + ((c.w ^ i) * 3 + i - 1) * 4096;
    ra = l0 + c.w * 3 * 4096;
    xo = opq(X_OFF + xb * XF_BYTES + (omt * 32 + 2 * ont) * 1024);
    so = opq(ST_OFF + xb * ST_BYTES + (omt * 16 + ont) * 256);
    sv = STW == 1 ? c.l31 * 8 + c.h * 4 : (c.lane < 16 ? c.l16 : OOB);
    asm volatile("" : "+v"(wa[0]), "+v"(wa[1]), "+v"(wa[2]), "+v"(ra), "+v"(sv));
  }
  DI void put(const f32x16& a, int i) {
#pragma unroll
    for (int q = 0; q < 4; ++q) *(LAS f32x4*)(uintptr_t)(unsigned)(wa[i - 1] + q * 1024) = f32x4{a[4 * q], a[4 * q + 1], a[4 * q + 2], a[4 * q + 3]};
  }
  f32x2 rf[8];
  DI void unpack(const v4u (&res)[2]) {
#pragma unroll
    for (int d = 0; d < 8; ++d) { const unsigned rx = res[d >> 2][d & 3]; rf[d] = f32x2{bflo(rx), bfhi(rx)}; }
    asm volatile("" : "+v"(rf[0]), "+v"(rf[1]), "+v"(rf[2]), "+v"(rf[3]), "+v"(rf[4]), "+v"(rf[5]), "+v"(rf[6]), "+v"(rf[7]));
  }
  template <typename HK> DI void sum(const f32x16& a0, v4u (&res)[2], HK&& hk) {
    BAR_LGKM();
    hk();
    f32x4 t[12];
#pragma unroll
    for (int q = 0; q < 4; ++q)
#pragma unroll
      for (int i = 0; i < 3; ++i) t[4 * i + q] = *(const LAS f32x4*)(uintptr_t)(unsigned)(ra + (4 * i + q) * 1024);
    SB();
#pragma unroll
    for (int hb = 0; hb < 2; ++hb) {
      f32x2 u[4], v[4];
      unsigned pr[4];
#pragma unroll
      for (int d = 0; d < 4; ++d) {
        const int q = 2 * hb + (d >> 1), e = 2 * (d & 1);
        u[d] = f32x2{a0[4 * q + e], a0[4 * q + e + 1]} + f32x2{t[q][e], t[q][e + 1]};
        v[d] = f32x2{t[4 + q][e], t[4 + q][e + 1]} + f32x2{t[8 + q][e], t[8 + q][e + 1]};
      }
      SB();
#pragma unroll
      for (int d = 0; d < 4; ++d) u[d] += v[d];
      SB();
#pragma unroll
      for (int d = 0; d < 4; ++d) { pr[d] = pk2(u[d][0], u[d][1]); asm volatile("" : "+v"(pr[d])); }
      SB();
#pragma unroll
      for (int d = 0; d < 4; ++d) u[d] = f32x2{bflo(pr[d]), bfhi(pr[d])};
      SB();
#pragma unroll
      for (int d = 0; d < 4; ++d) u[d] = rf[4 * hb + d] + u[d];
      SB();
#pragma unroll
      for (int d = 0; d < 4; ++d) res[hb][d] = pk2(u[d][0], u[d][1]);
      SB();
    }
  }
  DI void publish(const Ctx& c, const v4u (&res)[2]) {
    stx(c.rs, res[0], c.l16, xo);
    stx(c.rs, res[1], c.l16, xo + 1024);
    SB();
    float s0 = 0.f, s1 = 0.f, q0 = 0.f, q1 = 0.f;
#pragma unroll
    for (int d = 0; d < 4; ++d) {
      const unsigned a = res[0][d], b = res[1][d];
      s0 = dot2bf(a, 0x3f803f80u, s0); s1 = dot2bf(b, 0x3f803f80u, s1); q0 = dot2bf(a, a, q0); q1 = dot2bf(b, b, q1);
    }
    const auto r = __builtin_amdgcn_permlane32_swap(__builtin_bit_cast(unsigned, s0 + s1), __builtin_bit_cast(unsigned, q0 + q1), false, false);
    const unsigned r0 = r[0], r1 = r[1];
    const unsigned v = __builtin_bit_cast(unsigned, __builtin_bit_cast(float, r0) + __builtin_bit_cast(float, r1));
    if (STW == 1) __builtin_amdgcn_raw_buffer_store_b32(v, c.rs, sv, so, SC1);
    else {
      const auto p = __builtin_amdgcn_permlane32_swap(v, v, false, false);
      const unsigned p0 = p[0], p1 = p[1];
      const auto a = __builtin_amdgcn_permlane16_swap(p0, p0, false, false), b = __builtin_amdgcn_permlane16_swap(p1, p1, false, false);
      const unsigned a0 = a[0], a1 = a[1], b0 = b[0], b1 = b[1];
      stx(c.rs, v4u{a0, b0, a1, b1}, sv, so);
    }
  }
};
#define KS_LAST(CW, wA0, wA1, wB0, wB1, xA0, xA1, xB0, xB1) do { \
    MF(acc[1], CW, wA1, "v", xA0); MF(acc[2], CW, wA0, "v", xA1); MF(acc[3], CW, wA1, "v", xA1); SB(); \
    MF(acc[1], CW, wB1, "v", xB0); MF(acc[2], CW, wB0, "v", xB1); MF(acc[3], CW, wB1, "v", xB1); SB(); \
    MF(acc[0], CW, wA0, "v", xA0); asm volatile("s_nop 7"); SB(); \
    kt.put(acc[1], 1); SB(); \
    MF(acc[0], CW, wB0, "v", xB0); SB(); \
    kt.put(acc[2], 2); kt.put(acc[3], 3); SB(); } while (0)

struct GuOp { signed char t, pr, o; };
struct GuList {
  GuOp op[400];
  short first[134];
  int n;
};
__host__ __device__ constexpr int gu_dma(int z) { return z < 24 ? ((z & 1) ? -1 : z / 2) : (z < 48 && z % 3 == 1 ? 12 + (z - 25) / 3 : -1); }
__host__ __device__ constexpr int gu_wq(int z) { return z >= 49 && z <= 77 && z % 4 == 1 ? (z - 49) / 4 : (z >= 83 && z <= 103 && z % 5 == 3 ? 8 + (z - 83) / 5 : -1); }
#define WA 14
#define WB1 10
#define WB2 16
constexpr int GX = 40 - WA - WB1 - WB2, WG0 = WA + WB1 + WB2;
static_assert(GX >= 0 && GX <= 8 && WA % 2 == 0, "");
__host__ __device__ constexpr int gu_wd(int p) {
  return p >= 64 && p <= 78 && (p & 1) == 0 ? (p - 64) / 2 : (p >= 80 && p < 80 + 2 * GX && (p & 1) == 1 ? 16 + (p - 80) / 2 :
         (p == 94 || p == 96 || p == 98 ? 8 + (p - 94) / 2 : (p == 150 || p == 154 ? 11 + (p - 150) / 4 : (p >= 164 && p <= 166 ? 13 + p - 164 : -1))));
}
__host__ __device__ constexpr int gu_cost(int o) { return (o == 6 || o == 7 || o == 12 || o == 13) ? 8 : (o == 17 ? 12 : 4); }
__host__ __device__ constexpr GuList gu_build() {
  GuList L{};
  int n = 0;
  for (int t = 0; t < 6; ++t) {
    for (int pr = 0; pr < 4; ++pr) {
      if (t >= 5) { L.op[n++] = GuOp{(signed char)t, (signed char)pr, 0}; L.op[n++] = GuOp{(signed char)t, (signed char)pr, 1}; }
      L.op[n++] = GuOp{(signed char)t, (signed char)pr, 4};
      if (pr > 0) L.op[n++] = GuOp{(signed char)t, (signed char)(pr - 1), 15};
      for (int o = 5; o <= 14; ++o) {
        if (o == 6 && pr > 0) L.op[n++] = GuOp{(signed char)t, (signed char)(pr - 1), 16};
        if (o == 8 && t >= 5) { L.op[n++] = GuOp{(signed char)t, (signed char)pr, 2}; L.op[n++] = GuOp{(signed char)t, (signed char)pr, 3}; }
        L.op[n++] = GuOp{(signed char)t, (signed char)pr, (signed char)o};
      }
    }
    L.op[n++] = GuOp{(signed char)t, 3, 15}; L.op[n++] = GuOp{(signed char)t, 3, 16}; L.op[n++] = GuOp{(signed char)t, 3, 17};
  }
  L.n = n;
  int k = 0;
  for (int z = 0; z < 133; ++z) {
    L.first[z] = (short)k;
    int budget = gu_dma(z) >= 0 ? 8 : 16;
    while (k < n) {
      const int t = L.op[k].t, avail = t < 3 ? 1 : (t < 5 ? 68 : 101);
      if (z < avail || gu_cost(L.op[k].o) > budget) break;
      budget -= gu_cost(L.op[k].o);
      ++k;
    }
  }
  L.first[133] = (short)k;
  return L;
}
constexpr GuList GU_L = gu_build();
static_assert(GU_L.first[132] == GU_L.n, "the epilogues of tiles 0..5 fit into beta");
__host__ __device__ constexpr int gu_plan(int z) { return GU_L.first[z]; }
__host__ __device__ constexpr int gu_tile(int k) { return GU_L.op[k].t; }
__host__ __device__ constexpr int gu_pair(int k) { return GU_L.op[k].pr; }
__host__ __device__ constexpr int gu_op(int k) { return GU_L.op[k].o; }
__host__ __device__ constexpr int gu_stores(int z0, int z1) {
  int n = 0;
  for (int k = GU_L.first[z0]; k < GU_L.first[z1]; ++k) n += GU_L.op[k].o == 17;
  return n;
}
__host__ __device__ constexpr int gu_wq_in(int z0, int z1) {
  int n = 0;
  for (int z = z0; z < z1; ++z) n += gu_wq(z) >= 0 ? 2 : 0;
  return n;
}
__host__ __device__ constexpr int gu_young3() {
  int n = 0;
  for (int z = 49; z < 98; ++z) n += gu_wq(z) >= 0 && gu_stores(0, z + 1) >= 4 ? 2 : 0;
  return n;
}
#define PB0 2
#define PC0 7
#define PC1 11
#define EP1 1
#define GC0 81
#define GC1 86
#define GC2 91
#define DPRE 4
#define ZB 122
static_assert(gu_stores(0, ZB - 10) == 5 && gu_stores(0, ZB + 2) == 5 && gu_wq_in(ZB - 10, 132) == 0, "tile 4 is stored well before the second hand-off, tile 5 behind it");
static_assert(gu_stores(0, 94) == 4 && gu_stores(0, 98) == 4, "tile 3 is stored well before the early hand-off, tile 4 behind it");

extern "C" __global__ void __launch_bounds__(256, 1) k_all(const unsigned short* __restrict__ x, const unsigned short* __restrict__ wqkv,
                                                           const unsigned short* __restrict__ wo_, const unsigned short* __restrict__ wg,
                                                           const unsigned short* __restrict__ wu, const unsigned short* __restrict__ wd_,
                                                           unsigned char* __restrict__ ws, unsigned short* __restrict__ dout) {
  extern __shared__ __attribute__((aligned(16))) unsigned char smem[];
  Ctx c;
  c.lds = (LAS unsigned char*)smem;
  c.w = __builtin_amdgcn_readfirstlane(threadIdx.x >> 6);
  refresh(c);
  const int lds0 = (int)(unsigned)(uintptr_t)c.lds;
  v4u res[2];
  unsigned bl;
  AttS at;
  phase0(c, ws, x, wqkv, wo_, wg, wu, wd_, res, bl, at);
  int gq, jq, ja, ga, rb, sl, omt, ont;
  auto roles = [&]() {
    const int b = opq(blockIdx.x), bx = b & 7, bi = b >> 3;
    c.w = opq(__builtin_amdgcn_readfirstlane(c.w));
    gq = 4 * (bx >> 1) + (bi & 3); jq = 8 * (bx & 1) + (bi >> 2);
    ja = 2 * bx + (bi & 1); ga = bi >> 1;
    rb = 4 * bx + (bi & 3); sl = bi >> 2;
    omt = 2 * rb + (c.w >> 1); ont = 2 * sl + (c.w & 1);
    refresh(c);
  };
  roles();
#define ln15 (c.lane & 15)
#define w c.w
  STAMP(0);
  v4u wd[56];
#define wdo (WD_OFF + (sl * 112 + 28 * w) * 2048)
#define LDWD(i) wd[i] = ldw(c.rs, ((i) & 1) ? wv1 : wv0, wdo1 + dn_ks((i) >> 1) * 2048)
#define WD_BASES() const int wdo1 = opq(wdo); int wv0 = c.l16 + (w & 1) * 1024, wv1 = wv0 ^ 1024; asm volatile("" : "+v"(wv0), "+v"(wv1))

  auto att = [&](auto l0) {
    constexpr bool L0 = decltype(l0)::value;
    constexpr int layer = L0 ? 0 : 1;
    roles();
    unsigned gp, sent = (unsigned)opq(w);
    DmaRun dr;
    WD_BASES();
    unsigned pvw;
    v4u nan4;
    const int qo = opq(Q_OFF + (2 * ja + (w & 1)) * 65536 + (4 * ga + 2 * (w >> 1)) * 1024), ko = opq(K_OFF + layer * QKV_BYTES + (2 * ja + (w & 1)) * 65536);
    const int oo = opq(O_OFF + layer * XF_BYTES + ((4 * ga + 2 * (w >> 1)) * 32 + 2 * ja + (w & 1)) * 1024), oo1 = opq(oo + 32768);
    const int cx = opq(X_OFF + (2 * layer + 1) * XF_BYTES + (omt * 32 + 2 * ont) * 1024), cs = opq(ST_OFF + (2 * layer + 1) * ST_BYTES + (omt * 16 + ont) * 256);
    auto pf = [&](int i) {
      if (i == -2) { if (L0) pvw = poll_issue(c, 12); }
      else if (i == -3) {
        if (L0) { asm volatile("" : "+v"(pvw)); spin_f(c, 12, __builtin_amdgcn_readlane(bl, 12), pvw, __builtin_amdgcn_readlane(bl, 15), sent, 16u); }
        else WAIT_VM0();
      }
      else if (i == -1) { const int p0 = L0 ? 28 * w : 7 * w; dr.begin(opq(WG_OFF + jq * WG_SL + p0 * 1024), opq(lds0 + p0 * 1024)); }
      else if (i >= 200) {
        const int hh = i - 200;
        if (hh == CL0 - 1) { nan4 = v4u{~0u, ~0u, ~0u, ~0u}; asm volatile("" : "+v"(nan4)); }
        if (hh == CL0) CLR(oo, 0);
        if (hh == CL1) CLR(oo1, 0);
        if (hh == CL2) CLR(cx, 0);
        if (hh == CL3) CLR(cx, 1024);
        if (hh == CL4) CLR16(cs);
        if (hh == CL4 + 1) asm volatile("" :: "v"(nan4));
        if (hh == EA) { asm volatile("s_waitcnt vmcnt(4)" ::: "memory"); const Signal so_(c, 5 * layer + 2, bid_w(2 * ga + (ln15 >> 3), ln15 & 7), 16); so_.fire(c); }
      }
      else if (i >= 100) dr.piece(c.rs, c.l16, i - 100);
      else {
        dr.piece(c.rs, c.l16, i);
        if (i == 15) { int og = CNT_OFF + opq(blockIdx.x) * 128; asm volatile("global_load_dword %0, %1, %2 sc1" : "=v"(gp) : "v"(og), "s"(c.ws) : "memory"); }
        if (i == 27) {
          asm volatile("s_waitcnt vmcnt(%1)" : "+v"(gp) : "n"(12 + (CL2 > DS + 15) + (CL3 > DS + 15) + (CL4 > DS + 15)) : "memory");
          if (w == 0) {
            const int b15 = opq(blockIdx.x) & 15, bq = bid_qg(ln15, (jq & 14) + ((c.lane >> 4) & 1));
            const Signal sq(c, 13 + c.h, c.h ? b15 : bq, 0);
            const unsigned hi = (unsigned)opq(sent ? 0 : 1);
            go_wait(c, gp, __builtin_amdgcn_readlane(bl, 15));
            asm volatile("s_mov_b64 exec, %3\n\tglobal_atomic_add %0, %1, %2\n\ts_mov_b64 exec, -1" :: "v"(sq.off), "v"(sq.one), "s"(c.ws), "s"(((unsigned long long)hi << 32) | 0xffffffffull) : "memory");
          }
        }
      }
    };
    auto pw = [&](int i) { LDWD(2 * i); LDWD(2 * i + 1); };
    if (L0) {
      unsigned sv = 0u;
      const int pb = (opq(blockIdx.x) + 255) & 255, s4 = 4 * ((pb < 56 ? pb : pb - 56) >> 1), j0 = s4 / 7, j1 = (s4 + 3) / 7;
      const int bw = bid_w(c.l31, pb < 128 ? (pb - 112) >> 1 : pb & 7), bg = bid_qg(ln15, (c.lane & 16) ? j1 : j0);
      unsigned pv1 = 0u;
      if (EP1) pv1 = poll_issue(c, 1);
      sv = poll_at(c, SLOT_OFF + opq(blockIdx.x) * 4);
      {
        const Signal sj(c, 12, pb < 112 ? bg : bw, pb < 112 ? (j0 == j1 ? 16 : 32) : (pb < 184 ? 32 : 0));
        __builtin_amdgcn_s_waitcnt(0x0070);
        BAR_ALL();
        sj.fire(c);
      }
      if (pb == 255 && w == 2) {
        const v4u ma = *(const LAS v4u*)(c.lds + L_MA + c.l16);
        const int vm = SLOT_OFF + c.l16;
#pragma unroll 1
        for (int n = 0; n < 0x20000; ++n) {
          v4u mp;
          asm volatile("buffer_load_dwordx4 %0, %1, %2, 0 offen sc1\n\ts_waitcnt vmcnt(0)" : "=&v"(mp) : "v"(vm), "s"(c.rs) : "memory");
          const unsigned d = min(min(mp[0] - ma[0], mp[1] - ma[1]), min(mp[2] - ma[2], mp[3] - ma[3]));
          if (__builtin_amdgcn_ballot_w64(d < 2u) == 0ull) break;
        }
#pragma unroll
        for (int k = 0; k < 4; ++k) __hip_atomic_fetch_add((gu32*)(c.ws + CNT_OFF + (64 * k + c.lane) * 128), 1u, RLX_AGENT);
      }
      const unsigned p = __builtin_amdgcn_readlane(bl, 15), tg = __builtin_amdgcn_readlane(bl, 1);
      spin_f(c, 1, tg, w == 0 ? (EP1 ? (unsigned)opq((int)pv1) : tg - 1u) : tg, p, sent, 16u);
      if (w == 0 && opq((int)(sv - p)) == 2 && c.lane == 0) __hip_atomic_fetch_add((gu32*)(c.ws + SLOT_OFF + opq(blockIdx.x) * 4), 1u, RLX_AGENT);
    } else spin(c, 6, (unsigned)(min(ga, 2) + 1), w == 0 ? 0u : ~0u);
    if (!L0) __builtin_amdgcn_s_waitcnt(0x0F70);
    BAR_LGKM();
    STAMP(4 + layer * 20);
    phase_attn<L0, WA>(c, qo, ko, oo, at, pf, pw);
    roles();
    STAMP(5 + layer * 20);
    if (EA == 36) {
      const Signal so_(c, 5 * layer + 2, bid_w(2 * ga + (ln15 >> 3), ln15 & 7), 16);
      asm volatile("s_waitcnt vmcnt(%0)\n\ts_barrier" :: "n"(L0 ? DS + 27 - CL1 + 6 : (DS + 27 - CL1) / 4 + 5) : "memory");
      so_.fire(c);
    }
    STAMP(6 + layer * 20);
  };
  att(BoolC<true>{});

#pragma unroll 1
  for (int layer = 0;; ++layer) {
    const int u0 = layer * 5;
    roles();
    {
      v4u wo[16];
      const int woo = opq(WO_OFF + (sl * 32 + 8 * w) * 2048);
#pragma unroll
      for (int i = 0; i < 16; ++i) wo[i] = ldw(c.rs, c.l16, woo + (i & ~1) * 1024 + ((i ^ w) & 1) * 1024);
      if (layer == 0) {
        WD_BASES();
#pragma unroll
        for (int i = WA; i < WA + WB1; ++i) LDWD(i);
      }
      int ao = opq(O_OFF + layer * XF_BYTES + (omt * 32 + 8 * w) * 1024), ap = opq(O_OFF + layer * XF_BYTES + ((omt ^ 1) * 32 + 8 * w) * 1024);
      KTail kt;
      kt.init(c, L_RED_WO, 2 * layer + 1, omt, ont);
      kt.unpack(res);
      const Signal sx(c, u0 + 3, bid_qg(rb >> 1, ln15), 16);
      Signal sl_(c, 15 + layer, bid_w(2 * ga + (ln15 >> 3), ln15 & 7), 16);
      Chk ck;
      SB();
      wait_unit(c, u0 + 2, 16u);
      STAMP(7 + layer * 20);
      f32x16 acc[4];
#pragma unroll 1
      for (;;) {
      v4u xa[16];
      asm volatile("" : "+s"(ao), "+s"(ap), "+v"(c.l16));
#pragma unroll
      for (int k = 0; k < 5; ++k) { xa[2 * k] = ldx(c.rs, c.l16 + (k & 3) * 1024, ao + (k & 4) * 1024); SB(); xa[2 * k + 1] = ldx(c.rs, c.l16 + (k & 3) * 1024, ap + (k & 4) * 1024); SB(); }
#pragma unroll
      for (int k = 0; k < 6; ++k) {
        if (k == 0) {
          MF0(acc[0], "v", wo[0], "v", xa[0]); MF0(acc[1], "v", wo[1], "v", xa[0]); MF0(acc[2], "v", wo[0], "v", xa[1]); MF0(acc[3], "v", wo[1], "v", xa[1]);
        } else {
          MF(acc[0], "v", wo[2 * k], "v", xa[2 * k]); MF(acc[1], "v", wo[2 * k + 1], "v", xa[2 * k]);
          MF(acc[2], "v", wo[2 * k], "v", xa[2 * k + 1]); MF(acc[3], "v", wo[2 * k + 1], "v", xa[2 * k + 1]);
        }
        if (k + 5 < 8) { xa[2 * k + 10] = ldx(c.rs, c.l16, ao + (k + 5) * 1024); xa[2 * k + 11] = ldx(c.rs, c.l16, ap + (k + 5) * 1024); }
        if (WOF == 2 && k == 0 && ck.m) sx.fire(c);
        SB();
      }
      KS_LAST("v", wo[12], wo[13], wo[14], wo[15], xa[12], xa[13], xa[14], xa[15]);
      sl_.fire(c);
      if (__builtin_expect(ck.bad(acc[1][0], acc[2][0]) == 0ull, 1)) break;
      sl_.m = 0ull;
      ck.confirm(c, 15 + layer, 16u);
      }
      kt.sum(acc[0], res, [&]() { if (WOF == 1) sx.fire(c); });
      kt.publish(c, res);
      BAR_LGKM();
      if (WOF == 0) sx.fire(c);
    }
    roles();
    STAMP(8 + layer * 20);
    STAMP(9 + layer * 20);
    if (w > 0) dma_n<14>(c.rs, c.l16, WG_OFF + jq * WG_SL + (112 + 14 * (w - 1)) * 1024, lds0 + (112 + 14 * (w - 1)) * 1024);
    if (layer == 0) {
      WD_BASES();
#pragma unroll
      for (int i = WA + WB1; i < WG0; ++i) LDWD(i);
    }
    unsigned pv;
    int xo = opq(X_OFF + (2 * layer + 1) * XF_BYTES + (4 * gq + w) * 32 * 1024), sto = opq(ST_OFF + (2 * layer + 1) * ST_BYTES + (4 * gq + w) * 4096);
    LdsBases lb(c);
    Chk ck;
    Signal sl_(c, 17 + layer, bid_qg(rb >> 1, ln15), 16);
    asm volatile("" : "+v"(c.l31), "+v"(c.h));
    int fx = opq(layer ? OOB : X_OFF + 2 * XF_BYTES + (omt * 32 + 2 * ont) * 1024), fs = opq(layer ? OOB : ST_OFF + 2 * ST_BYTES + (omt * 16 + ont) * 256);
    int rso = opq(WG_OFF + jq * WG_SL + (154 + 4 * w) * 1024);
    const int rl = opq(lds0 + 4 * w * 1024), rlast = opq((w == 3 ? 13 : 16) * 1024);
    const int wqo = opq(WQ_OFF + jq * WQ_SL + 4 * w * 1024);
    int wdo1 = opq(wdo), wv0 = c.l16 + (w & 1) * 1024, wv1 = wv0 ^ 1024;
    asm volatile("" : "+v"(wv0), "+v"(wv1));
    const int ho = opq(H_OFF + ((4 * gq + w) * 112 + 7 * jq) * 1024);
    wait_unit(c, u0 + 3, 16u);
    STAMP(10 + layer * 20);
    {
      constexpr int XW = 10, XA = 6;
      v4u xa[32];
      v2u sv[8];
      f32x16 acc[7];
      v4u wa[8];
      v4u xm, nan4;
      int l16m;
      unsigned pq;
      StatSteps ss;
      f32x16 accb[2];
#define WN(p, i) wa[((p) + (i) < 231 ? (p) + (i) : (p)) & 7]
#define GU_MF1(M, M0, A, first, ks, p, ...) do { \
        if (first) { if ((ks) < XA) M0(A, "v", wa[(p) & 7], "a", xa[(ks) < 32 ? (ks) : 0], ##__VA_ARGS__); else M0(A, "v", wa[(p) & 7], "v", xa[(ks) < 32 ? (ks) : 0], ##__VA_ARGS__); } \
        else if ((ks) == 32) M(A, "v", wa[(p) & 7], "v", xm, ##__VA_ARGS__); \
        else if ((ks) < XA) M(A, "v", wa[(p) & 7], "a", xa[(ks) < 32 ? (ks) : 0], ##__VA_ARGS__); \
        else M(A, "v", wa[(p) & 7], "v", xa[(ks) < 32 ? (ks) : 0], ##__VA_ARGS__); } while (0)
#define GU_MF(A, first, ks, p) do { \
        if (((p) & 3) == 0) GU_MF1(MFW, MF0W, A, first, ks, p, WN(p, 1), WN(p, 2), WN(p, 3)); else GU_MF1(MF, MF0, A, first, ks, p); \
        SB(); \
        if ((p) + 7 < 231) wa[((p) + 7) & 7] = ldsr(lb, ((p) + 7 < 154 ? (p) + 7 : (p) + 7 - 154) * 1024); \
        SB(); } while (0)
#pragma unroll 1
      for (;;) {
      asm volatile("" : "+s"(xo), "+s"(sto), "+v"(c.l16), "+v"(c.l31), "+v"(c.h), "+v"(c.lane), "+v"(lb.b0), "+v"(lb.b1), "+v"(lb.b2), "+s"(wdo1), "+v"(wv0), "+v"(wv1), "+s"(fx), "+s"(fs));
#pragma unroll
      for (int ks = 0; ks < XW; ++ks) { xa[ks] = ldx(c.rs, c.l16 + (ks & 3) * 1024, xo + (ks & ~3) * 1024); SB(); }
#pragma unroll
      for (int i = 0; i < 7; ++i) wa[i] = ldsr(lb, i * 1024);
      SB();
#pragma clang loop unroll(full)
      for (int ks = 0; ks < 33; ++ks)
#pragma clang loop unroll(full)
      for (int i = 0; i < 3; ++i) {
        const int p = ks * 3 + i;
        GU_MF(acc[i], ks == 0, ks, p);
        if (i == 0 && XW + ks < 32) { const int k = XW + ks; xa[k] = ldx(c.rs, c.l16 + (k & 3) * 1024, xo + (k & ~3) * 1024); }
        if (i > 0 && ks >= 12 && ks < 16) sv[2 * (ks - 12) + i - 1] = ld_stat(c, sto, 2 * (ks - 12) + i - 1);
        if (gu_wd(p) >= 0 && layer == 0) LDWD(WG0 + gu_wd(p));
        if (p >= 72 && p < 72 + 13) ss.step(p - 72, sv, c.h);
        if (p == 87) { xm = mean_frag(ss.mean); asm volatile("" : "+v"(xm)); }
        if (p == 17) pq = poll_issue(c, 13);
        if (p == 71) pq = (unsigned)opq((int)pq);
        if (p == 95) sl_.fire(c);
        if (p == GC0 - 2) { nan4 = v4u{~0u, ~0u, ~0u, ~0u}; l16m = c.lane < 16 ? c.l16 : OOB; asm volatile("" : "+v"(nan4), "+v"(l16m)); }
        if (p == GC0 || p == GC1) __builtin_amdgcn_raw_buffer_store_b128(nan4, c.rs, c.l16 + (p == GC1) * 1024, fx, SC1);
        if (p == GC2) __builtin_amdgcn_raw_buffer_store_b128(nan4, c.rs, l16m, fs, SC1);
        SB();
      }
      asm volatile("s_nop 6");
      SB();
      if (__builtin_expect(ck.bad(acc[0][0], ss.rstd) == 0ull, 1)) break;
      sl_.m = 0ull;
      ck.confirm(c, 17 + layer, 16u);
      }
      const float c1 = -ss.rstd * LOG2E, ic2 = ss.sd * ss.sd;
      float g0, g1, e0, e1, e2, e3, p0, p1;
      unsigned hp[4];
#define GU_ST(t) stx(c.rs, v4u{hp[0], hp[1], hp[2], hp[3]}, c.l16, ho + (t) * 1024)
      auto epi = [&](int k) {
        const int t = gu_tile(k), o = gu_op(k), r = 2 * gu_pair(k);
#define PIN(v) asm volatile("" : "+v"(v))
        if (o == 0) { g0 = acc[t][r] + accb[t - 5][r]; PIN(g0); }
        if (o == 1) { g1 = acc[t][r + 1] + accb[t - 5][r + 1]; PIN(g1); }
        if (o == 2) { p0 = acc[t][8 + r] + accb[t - 5][8 + r]; PIN(p0); }
        if (o == 3) { p1 = acc[t][9 + r] + accb[t - 5][9 + r]; PIN(p1); }
        if (o == 4) { e0 = (t >= 5 ? g0 : acc[t][r]) * c1; PIN(e0); }
        if (o == 5) { e2 = (t >= 5 ? g1 : acc[t][r + 1]) * c1; PIN(e2); }
        if (o == 6) { e0 = EX2(e0); PIN(e0); }
        if (o == 7) { e1 = EX2(e2); PIN(e1); }
        if (o == 8) { p0 = t >= 5 ? g0 * p0 : acc[t][r] * acc[t][8 + r]; PIN(p0); }
        if (o == 9) { p1 = t >= 5 ? g1 * p1 : acc[t][r + 1] * acc[t][9 + r]; PIN(p1); }
        if (o == 10) { e0 = __builtin_fmaf(e0, ic2, ic2); PIN(e0); }
        if (o == 11) { e1 = __builtin_fmaf(e1, ic2, ic2); PIN(e1); }
        if (o == 12) { e3 = __builtin_amdgcn_rcpf(e0); PIN(e3); }
        if (o == 13) { e1 = __builtin_amdgcn_rcpf(e1); PIN(e1); }
        if (o == 14) { p0 *= e3; PIN(p0); }
        if (o == 15) { p1 *= e1; PIN(p1); }
        if (o == 16) { hp[r >> 1] = pk2(p0, p1); PIN(hp[r >> 1]); }
        if (o == 17) GU_ST(t);
      };
#pragma clang loop unroll(full)
      for (int zz = 0; zz < 4; ++zz)
#pragma clang loop unroll(full)
      for (int zy = 0; zy < 33; ++zy) {
        const int z = zz * 33 + zy, p = 99 + z;
        if (z == 0) __builtin_amdgcn_s_barrier();
        if (z == 48) {
          spin(c, 13, 32u, w == 0 ? pq : ~0u);
          asm volatile("s_waitcnt vmcnt(%0)\n\ts_barrier" :: "n"(8 + gu_stores(22, 48)) : "memory");
        }
        if (z == 82) {
          if (layer == 0) asm volatile("s_waitcnt vmcnt(%0)" :: "n"(gu_stores(46, 82) + gu_wq_in(46, 82) + 5) : "memory"); else asm volatile("s_waitcnt vmcnt(%0)" :: "n"(gu_stores(46, 82)) : "memory");
          __builtin_amdgcn_s_barrier();
        }
        if (z == 98) {
          if (layer == 0) asm volatile("s_waitcnt vmcnt(%0)" :: "n"(gu_young3()) : "memory"); else asm volatile("s_waitcnt vmcnt(0)" ::: "memory");
          const Signal sg(c, 10 + layer, bid_w(2 * gq + (ln15 >> 3), ln15 & 7), 16);
          __builtin_amdgcn_s_barrier();
          sg.fire(c);
        }
        if (z == ZB) {
#pragma unroll
          for (int i = 0; i < 56; i += 8)
            asm volatile("" : "+a"(wd[i]), "+a"(wd[i + 1]), "+a"(wd[i + 2]), "+a"(wd[i + 3]), "+a"(wd[i + 4]), "+a"(wd[i + 5]), "+a"(wd[i + 6]), "+a"(wd[i + 7]));
        }
        if (z == ZB + 1) { int og = CNT_OFF + ((10 + layer) * 256 + opq(blockIdx.x)) * 128; asm volatile("global_load_dword %0, %1, %2 sc1" : "=v"(pv) : "v"(og), "s"(c.ws) : "memory"); }
        if (z < 66) GU_MF(acc[3 + (z & 1)], z < 2, z >> 1, p);
        else if (zy & 1) GU_MF(accb[zz - 2], zy == 1, zy, p);
        else GU_MF(acc[3 + zz], zy == 0, zy, p);
        if (gu_dma(z) >= 0) {
          const int n = gu_dma(z);
          if (n == 0) asm volatile("s_mov_b32 m0, %0\n\ts_nop 0" :: "s"(rl));
          if ((n & 3) < 3 || n == 19) asm volatile("buffer_load_dwordx4 %0, %1, %2 offen offset:%3" WSCS " lds" :: "v"(c.l16), "s"(c.rs), "s"(rso), "n"((n & 3) * 1024) : "memory");
          else if (n < 15) asm volatile("buffer_load_dwordx4 %1, %2, %0 offen offset:3072" WSCS " lds\n\ts_add_u32 m0, m0, 0x4000\n\ts_add_u32 %0, %0, 0x4000" : "+s"(rso) : "v"(c.l16), "s"(c.rs) : "memory", "scc");
          else asm volatile("buffer_load_dwordx4 %1, %2, %0 offen offset:3072" WSCS " lds\n\ts_add_u32 m0, m0, %3\n\ts_add_u32 %0, %0, %3" : "+s"(rso) : "v"(c.l16), "s"(c.rs), "s"(rlast) : "memory", "scc");
        }
#pragma unroll
        for (int k = gu_plan(z); k < gu_plan(z + 1); ++k) epi(k);
        if (gu_wd(p) >= 0 && layer == 0) LDWD(WG0 + gu_wd(p));
        if (gu_wq(z) >= 0 && layer == 0) {
          const int n = gu_wq(z) >> 1, hf = gu_wq(z) & 1;
          if (n < 6) {
            const int lq = n < 4 ? rl + (78 + 16 * n) * 1024 : (n == 4 ? rl + (w == 3 ? -12 : 142) * 1024 : rl + 4 * 1024);
            asm volatile("s_mov_b32 m0, %3\n\ts_nop 0\n\tbuffer_load_dwordx4 %0, %1, %2 offen offset:%4" WSCS " lds\n\tbuffer_load_dwordx4 %0, %1, %2 offen offset:%5" WSCS " lds"
                         :: "v"(c.l16), "s"(c.rs), "s"(wqo + n * 16384), "s"(lq), "n"(hf * 2048), "n"(hf * 2048 + 1024) : "memory");
          } else dma<1>(c.rs, c.l16, wqo + (96 - 3 * w) * 1024, lds0 + (20 + w) * 1024);
        }
        SB();
      }
      NOP12();
      STAMP(11 + layer * 20);
      {
        float ev[8], pr[8];
#pragma unroll
        for (int r = 0; r < 8; ++r) {
          const float g = acc[6][r] + accb[1][r];
          ev[r] = __builtin_amdgcn_rcpf(__builtin_fmaf(EX2(g * c1), ic2, ic2));
          pr[r] = g * (acc[6][8 + r] + accb[1][8 + r]);
        }
#pragma unroll
        for (int r = 0; r < 8; r += 2) hp[r >> 1] = pk2(pr[r] * ev[r], pr[r + 1] * ev[r + 1]);
        GU_ST(6);
      }
    }
    roles();
    STAMP(12 + layer * 20);
    asm volatile("s_waitcnt vmcnt(2)" : "+v"(pv) :: "memory");
    wave_wait(c, 10 + layer, 16u, pv);
    {
      const int ao = opq(H_OFF + (omt * 112 + 28 * w) * 1024), ap = opq(H_OFF + ((omt ^ 1) * 112 + 28 * w) * 1024);
      constexpr int XW = 8;
      v4u xa[2 * XW];
#define DN_LD(k) do { xa[2 * ((k) % XW)] = ldx(c.rs, c.l16, ao + dn_ks(k) * 1024); xa[2 * ((k) % XW) + 1] = ldx(c.rs, c.l16, ap + dn_ks(k) * 1024); } while (0)
      const Signal sg(c, u0 + 4, bid_w(2 * gq + (ln15 >> 3), ln15 & 7), 16);
#pragma unroll
      for (int k = 0; k < DPRE; ++k) DN_LD(k);
      SB();
      STAMP(13 + layer * 20);
      asm volatile("s_waitcnt vmcnt(%0)\n\ts_barrier" :: "n"(2 * DPRE) : "memory");
      sg.fire(c);
      SB();
#pragma unroll
      for (int k = DPRE; k < XW; ++k) DN_LD(k);
      STAMP(14 + layer * 20);
      SB();
      f32x16 acc[4];
      KTail kt;
#pragma unroll
      for (int k = 0; k < 26; ++k) {
        const int q = k % XW;
        if (k == 0) {
          MF0(acc[0], "a", wd[0], "v", xa[0]); MF0(acc[1], "a", wd[1], "v", xa[0]); MF0(acc[2], "a", wd[0], "v", xa[1]); MF0(acc[3], "a", wd[1], "v", xa[1]);
        } else {
          MF(acc[0], "a", wd[2 * k], "v", xa[2 * q]); MF(acc[1], "a", wd[2 * k + 1], "v", xa[2 * q]);
          MF(acc[2], "a", wd[2 * k], "v", xa[2 * q + 1]); MF(acc[3], "a", wd[2 * k + 1], "v", xa[2 * q + 1]);
        }
        if (k == PB0) (void)0;
        if (k < 8) DN_LD(k + XW);
        if (k == PC0) pv = poll_issue(c, u0 + 4);
        if (k == PC1) {
          SB();
          wave_wait(c, u0 + 4, 16u, pv);
#pragma unroll
          for (int n = 16; n < PC1 + XW; ++n) DN_LD(n);
        }
        if (k >= PC1 && k + XW < 28) DN_LD(k + XW);
        if (k == 22) { roles(); kt.init(c, L_RED_DN, 2, omt, ont); }
        if (k == 24) kt.unpack(res);
        if (DNF == 2 && k == 23 && layer == 0) { const Signal sd_(c, 5, bid_qg(rb >> 1, ln15), 16); sd_.fire(c); }
        SB();
      }
      const Signal sd(c, 5, bid_qg(rb >> 1, ln15), 16);
      KS_LAST("a", wd[52], wd[53], wd[54], wd[55], xa[2 * (26 % XW)], xa[2 * (26 % XW) + 1], xa[2 * (27 % XW)], xa[2 * (27 % XW) + 1]);
      kt.sum(acc[0], res, [&]() { if (DNF == 1 && layer == 0) sd.fire(c); });
      if (layer == 0) {
        kt.publish(c, res);
        STAMP(15);
        BAR_LGKM();
        if (DNF == 0) sd.fire(c);
        STAMP(16);
        if (w > 0) dma_n<14>(c.rs, c.l16, WG_OFF + jq * WG_SL + (14 + 14 * w) * 1024, lds0 + (14 + 14 * w) * 1024);
        Chk cq;
        Signal sq_(c, 19, bid_qg(rb >> 1, ln15), 16);
        wait_unit(c, 5, 16u);
        STAMP(21);
#pragma unroll 1
        for (;;) {
          roles();
          const unsigned long long bad = phase_qkv(c, 1, gq, jq, cq);
          sq_.fire(c);
          if (__builtin_expect(bad == 0ull, 1)) break;
          sq_.m = 0ull;
          cq.confirm(c, 19, 16u);
        }
        roles();
        STAMP(22);
        signal_unit(c, 6, bid_att(gq + ln15, jq), min(3, 16 - gq));
        STAMP(23);
        if (w > 0) dma_n<14>(c.rs, c.l16, WG_OFF + jq * WG_SL + (56 + 14 * w) * 1024, lds0 + (56 + 14 * w) * 1024);
        attn_setup(c, 1, ga, ja, at);
        att(BoolC<false>{});
      } else {
        unsigned short* dp = dout + (size_t)(omt * 32 + c.l31) * D_ + ont * 32 + 8 * c.h;
        *(v4u*)dp = res[0];
        *(v4u*)(dp + 16) = res[1];
        STAMP(35);
        break;
      }
    }
  }
}

#undef w
extern "C" void kernel_launch(void* const* d_in, const int* in_sizes, int n_in, void* d_out, int out_size, void* d_ws, size_t ws_size,
                              hipStream_t stream) {
  const unsigned short* x = (const unsigned short*)d_in[0];
  const unsigned short* wqkv = (const unsigned short*)d_in[1];
  const unsigned short* wo = (const unsigned short*)d_in[2];
  const unsigned short* wg = (const unsigned short*)d_in[4];
  const unsigned short* wu = (const unsigned short*)d_in[5];
  const unsigned short* wd = (const unsigned short*)d_in[6];
  unsigned char* ws = (unsigned char*)d_ws;
  (void)hipFuncSetAttribute((const void*)k_all, hipFuncAttributeMaxDynamicSharedMemorySize, LDS_BYTES);
  k_all<<<256, 256, LDS_BYTES, stream>>>(x, wqkv, wo, wg, wu, wd, ws, (unsigned short*)d_out);
}
```
